```python
import torch

def _softmax(x, axis=-1):
    return torch.softmax(x, dim=axis)
def _gelu(x):
    return torch.nn.functional.gelu(x, approximate="tanh")
def _silu(x):
    return torch.nn.functional.silu(x)
def _sigmoid(x):
    return torch.sigmoid(x)
def _layernorm(x, axis=-1, eps=1e-5):
    m = x.mean(dim=axis, keepdim=True)
    v = x.var(dim=axis, keepdim=True, unbiased=False)
    return (x - m) / torch.sqrt(v + eps)
def _scatter_add(tgt, idx, src, axis=0):
    return tgt.clone().index_add_(axis, idx, src)
def _segment_sum(src, seg, n):
    out = torch.zeros((n,) + src.shape[1:], dtype=src.dtype)
    return out.index_add_(0, seg, src)
def _pad(x, pw):
    flat = tuple(v for pair in reversed(pw) for v in pair)
    return torch.nn.functional.pad(x, flat)

kernel_name = 'parallel_sum__ssm_conv_s4__dense_activation_gelu__prompt'

def setup_inputs():

    _w = (lambda a: (lambda t: t.to(torch.bfloat16) if t.is_floating_point() else t)(torch.as_tensor(a)))
    def _A_setup():
        B, L, D, N = 1, 2048, 640, 128
        A_re = -(lambda _x: torch.exp(torch.as_tensor(_x)))((lambda *s: torch.randn(*s, dtype=torch.bfloat16) * 6.4)(D, N))
        A_im = torch.pi * (lambda *s: torch.randn(*s, dtype=torch.bfloat16) * 0.05)(D, N)
        B_re = (lambda *s: torch.randn(*s, dtype=torch.bfloat16) * 0.1)(D, N)
        B_im = (lambda *s: torch.randn(*s, dtype=torch.bfloat16) * 0.1)(D, N)
        C_re = (lambda *s: torch.randn(*s, dtype=torch.bfloat16) * 0.1)(D, N)
        C_im = (lambda *s: torch.randn(*s, dtype=torch.bfloat16) * 0.1)(D, N)
        log_dt = (lambda *s: torch.randn(*s, dtype=torch.bfloat16) * 0.05)(D) * 0.5 - 4.0
        Dskip = (lambda *s: torch.randn(*s, dtype=torch.bfloat16) * 0.05)(D)
        return {
            "u": _w((lambda *s: torch.randn(*s, dtype=torch.bfloat16) * 0.05)(B, L, D)),
            "A_re": _w(A_re),
            "A_im": _w(A_im),
            "B_re": _w(B_re),
            "B_im": _w(B_im),
            "C_re": _w(C_re),
            "C_im": _w(C_im),
            "log_dt": _w(log_dt),
            "Dskip": _w(Dskip),
        }
    _A_in = _A_setup()

    def _B_setup():
        B, S, D, F = 1, 2048, 640, 1728
        return {
            "x": _w((lambda *s: torch.randn(*s, dtype=torch.bfloat16) * 0.05)(B, S, D)),
            "w1": _w((lambda *s: torch.randn(*s, dtype=torch.bfloat16) * 0.8)(D, F) * (D ** -0.5)),
            "b1": _w((lambda *s: torch.randn(*s, dtype=torch.bfloat16) * 0.2)(F) * 0.01),
            "w2": _w((lambda *s: torch.randn(*s, dtype=torch.bfloat16) * 0.2)(F, D) * (F ** -0.5)),
            "b2": _w((lambda *s: torch.randn(*s, dtype=torch.bfloat16) * 0.2)(D) * 0.01),
        }
    _B_in = _B_setup()

    return {
        "A_u": _A_in["u"],
        "A_A_re": _A_in["A_re"],
        "A_A_im": _A_in["A_im"],
        "A_B_re": _A_in["B_re"],
        "A_B_im": _A_in["B_im"],
        "A_C_re": _A_in["C_re"],
        "A_C_im": _A_in["C_im"],
        "A_log_dt": _A_in["log_dt"],
        "A_Dskip": _A_in["Dskip"],
        "B_x": _B_in["x"],
        "B_w1": _B_in["w1"],
        "B_b1": _B_in["b1"],
        "B_w2": _B_in["w2"],
        "B_b2": _B_in["b2"],
    }

def reference(A_u, A_A_re, A_A_im, A_B_re, A_B_im, A_C_re, A_C_im, A_log_dt, A_Dskip, B_x, B_w1, B_b1, B_w2, B_b2):
    def _A_ref(u, A_re, A_im, B_re, B_im, C_re, C_im, log_dt, Dskip):
        Bsz, L, D = u.shape
        dt = torch.exp(log_dt)[:, None]
        A = A_re + 1j * A_im
        Bc = B_re + 1j * B_im
        Cc = C_re + 1j * C_im
        logdA = dt * A
        dB = dt * Bc
        l = (lambda x, dt: x.to(dt))(torch.arange(L), A_re.dtype)
        P = torch.exp(logdA[:, :, None] * l[None, None, :])
        K = torch.einsum("dn,dnl,dn->dl", Cc, P, dB).real
        L2 = 2 * L
        Kp = _pad(K, ((0, 0), (0, L)))
        up = (lambda x, *d: x.permute(*d))(u, 0, 2, 1)
        up = _pad(up, ((0, 0), (0, 0), (0, L)))
        Kf = (lambda x, axes=(-2,-1): torch.fft.rfft2(x.float(), dim=axes))(Kp[:, None, :], axes=(-2, -1))
        Uf = (lambda x, axes=(-2,-1): torch.fft.rfft2(x.float(), dim=axes))(up[:, :, None, :], axes=(-2, -1))
        Yf = Uf * Kf[None, :, :, :]
        y = (lambda x, s, axes=(-2,-1): torch.fft.irfft2(x, s=s, dim=axes).to(torch.bfloat16))(Yf, (1, L2), axes=(-2, -1))[:, :, 0, :L]
        y = (lambda x, *d: x.permute(*d))(y, 0, 2, 1) + u * Dskip[None, None, :]
        return y

    def _B_ref(x, w1, b1, w2, b2):
        h = torch.einsum("bsd,df->bsf", x, w1) + b1
        h = _gelu(h)
        return torch.einsum("bsf,fd->bsd", h, w2) + b2

    x = A_u
    xn = _layernorm(x, axis=-1)
    return x + _A_ref(xn, A_A_re, A_A_im, A_B_re, A_B_im, A_C_re, A_C_im, A_log_dt, A_Dskip) + _B_ref(xn, B_w1, B_b1, B_w2, B_b2)
```

```cpp
#include <hip/hip_runtime.h>
#include <hip/hip_bf16.h>
#include <stdint.h>

constexpr int L = 2048, D = 640, N = 128, F = 1728;
typedef unsigned short u16;
__device__ __forceinline__ float bf2f(u16 v) { return __uint_as_float((unsigned)v << 16); }
__device__ __forceinline__ u16 f2bf(float f) { __hip_bfloat16 b = __float2bfloat16(f); return *(u16*)&b; }
__device__ __forceinline__ float rbf(float f) { return bf2f(f2bf(f)); }

__global__ void k_ln(const u16* x, u16* xn) {
  int row = blockIdx.x; int tid = threadIdx.x;
  float v[10]; float s = 0;
  for (int i = 0; i < 10; i++) { v[i] = bf2f(x[row * D + tid + 64 * i]); s += v[i]; }
  for (int m = 32; m; m >>= 1) s += __shfl_xor(s, m);
  float mean = s / D; float q = 0;
  for (int i = 0; i < 10; i++) { float d = v[i] - mean; q += d * d; }
  for (int m = 32; m; m >>= 1) q += __shfl_xor(q, m);
  float var = rbf(q / D); float mb = rbf(mean);
  float sd = rbf(sqrtf(rbf(var + 1e-5f)));
  for (int i = 0; i < 10; i++) xn[row * D + tid + 64 * i] = f2bf(rbf(v[i] - mb) / sd);
}

__global__ void k_kgen(const u16* Are, const u16* Aim, const u16* Bre, const u16* Bim, const u16* Cre, const u16* Cim,
                       const u16* logdt, float* K) {
  int d = blockIdx.y; int l = blockIdx.x * blockDim.x + threadIdx.x;
  float dt = rbf(expf(bf2f(logdt[d])));
  float lf = rbf((float)l);
  float acc = 0;
  for (int n = 0; n < N; n++) {
    float zr = dt * bf2f(Are[d * N + n]), zi = dt * bf2f(Aim[d * N + n]);
    float br = dt * bf2f(Bre[d * N + n]), bi = dt * bf2f(Bim[d * N + n]);
    float cr = bf2f(Cre[d * N + n]), ci = bf2f(Cim[d * N + n]);
    float wr = cr * br - ci * bi, wi = cr * bi + ci * br;
    float e = expf(zr * lf); float sn, cs; sincosf(zi * lf, &sn, &cs);
    acc += e * (wr * cs - wi * sn);
  }
  K[d * L + l] = acc;
}

__global__ void k_conv(const u16* xn, const float* K, float* y) {
  int d = blockIdx.x; __shared__ float su[L]; __shared__ float sk[L];
  for (int i = threadIdx.x; i < L; i += blockDim.x) { su[i] = bf2f(xn[i * D + d]); sk[i] = K[d * L + i]; }
  __syncthreads();
  for (int t = threadIdx.x; t < L; t += blockDim.x) { float a = 0; for (int s = 0; s <= t; s++) a += sk[t - s] * su[s]; y[t * D + d] = a; }
}

__global__ void k_gemm1(const u16* xn, const u16* w1, const u16* b1, u16* h) {
  int f = blockIdx.x * blockDim.x + threadIdx.x; int t = blockIdx.y; if (f >= F) return;
  float a = 0; for (int k = 0; k < D; k++) a += bf2f(xn[t * D + k]) * bf2f(w1[k * F + f]);
  float hv = rbf(rbf(a) + bf2f(b1[f]));
  float inner = 0.7978845608028654f * (hv + 0.044715f * hv * hv * hv);
  h[t * F + f] = f2bf(0.5f * hv * (1.f + tanhf(inner)));
}

__global__ void k_gemm2(const u16* h, const u16* w2, const u16* b2, const u16* x, const u16* xn, const float* y, const u16* dsk, u16* out) {
  int d = blockIdx.x * blockDim.x + threadIdx.x; int t = blockIdx.y; if (d >= D) return;
  float a = 0; for (int k = 0; k < F; k++) a += bf2f(h[t * F + k]) * bf2f(w2[k * D + d]);
  float yb = rbf(rbf(a) + bf2f(b2[d]));
  float ya = rbf(rbf(y[t * D + d]) + rbf(bf2f(xn[t * D + d]) * bf2f(dsk[d])));
  out[t * D + d] = f2bf(rbf(bf2f(x[t * D + d]) + ya) + yb);
}

extern "C" void kernel_launch(void* const* d_in, const int* in_sizes, int n_in, void* d_out, int out_size, void* d_ws, size_t ws_size,
                              hipStream_t stream) {
  const u16* x = (const u16*)d_in[0];
  char* ws = (char*)d_ws;
  u16* xn = (u16*)ws; float* K = (float*)(ws + (4 << 20)); float* y = (float*)(ws + (16 << 20)); u16* h = (u16*)(ws + (32 << 20));
  k_ln<<<L, 64, 0, stream>>>(x, xn);
  k_kgen<<<dim3(L / 256, D), 256, 0, stream>>>((const u16*)d_in[1], (const u16*)d_in[2], (const u16*)d_in[3], (const u16*)d_in[4],
                                              (const u16*)d_in[5], (const u16*)d_in[6], (const u16*)d_in[7], K);
  k_conv<<<D, 256, 0, stream>>>(xn, K, y);
  k_gemm1<<<dim3((F + 255) / 256, L), 256, 0, stream>>>(xn, (const u16*)d_in[10], (const u16*)d_in[11], h);
  k_gemm2<<<dim3((D + 255) / 256, L), 256, 0, stream>>>(h, (const u16*)d_in[12], (const u16*)d_in[13], x, xn, y, (const u16*)d_in[8], (u16*)d_out);
}
```

```cpp
#include <hip/hip_runtime.h>
#include <hip/hip_bf16.h>
#include <stdint.h>

namespace {
typedef unsigned short u16;
typedef unsigned long long u64;
typedef __bf16 bf8 __attribute__((ext_vector_type(8)));
typedef __bf16 bf2 __attribute__((ext_vector_type(2)));
typedef short s4 __attribute__((ext_vector_type(4)));
typedef float f16v __attribute__((ext_vector_type(16)));
typedef float f4 __attribute__((ext_vector_type(4)));
typedef float f2 __attribute__((ext_vector_type(2)));
typedef unsigned v4u __attribute__((ext_vector_type(4)));
typedef unsigned v2u __attribute__((ext_vector_type(2)));
#define LAS __attribute__((address_space(3)))
#define GAS __attribute__((address_space(1)))
#define DI __device__ __forceinline__

constexpr int L = 2048, D = 640, N = 128, F = 1728;
constexpr unsigned WS_FLAG = 4096;
constexpr int FL_H = 0, FL_X = 224, FL_Y = 384, FL_W = 608;
constexpr unsigned WS_ST = 256u << 10;
constexpr unsigned WS_CS = WS_ST + 2048 * 8;
constexpr unsigned WS_HT = 1u << 20;
constexpr unsigned WS_W2T = WS_HT + 128u * 54 * 1024;
constexpr unsigned WS_XT = WS_W2T + 40u * 54 * 1024;
constexpr unsigned WS_YT = WS_XT + 640u * 4096;
constexpr unsigned WS_END = WS_YT + 640u * 4096;
constexpr unsigned WS_DBG = 32u << 20;
constexpr unsigned OOB = 0x40000000u;
constexpr int LDS_BYTES = 163840;
constexpr int SLOT = 32768;
constexpr int TAB = 98304, TROW = 272, TCH = 57 * TROW;
constexpr int W2U = TAB + 3 * TCH;
constexpr int CNT = W2U + 4352;
constexpr int TAGL = CNT + 240;
constexpr int KARR = 150016, KCH = 4608;
constexpr int COLT = 131072;
constexpr int STT = COLT + 2048;
constexpr int UCP = 65536, UST = 4160, UCH = 4 * UST;
constexpr int GT = 1088, GW = 5 * GT, GBUF = 8 * GW, YT2 = 2 * GBUF, YT2ROW = 168;
constexpr unsigned SPIN_CAP = 1u << 17;
#define SLP 2

DI float bflo(unsigned v) { return __uint_as_float(v << 16); }
DI float bfhi(unsigned v) { return __uint_as_float(v & 0xffff0000u); }
DI f2 unpk(unsigned v) { return f2{bflo(v), bfhi(v)}; }
DI unsigned pk2(f2 p) { return __builtin_bit_cast(unsigned, __builtin_convertvector(p, bf2)); }
DI unsigned pk(float a, float b) { return pk2(f2{a, b}); }
DI float rbf(float a) { return bflo(pk(a, a)); }
DI bf8 asbf(v4u v) { return __builtin_bit_cast(bf8, v); }
DI v4u mk_srd(const void* p, unsigned bytes) { u64 a = (u64)p; return v4u{(unsigned)a, (unsigned)(a >> 32) & 0xffffu, bytes, 0x00020000u}; }
#define SET_M0(v) asm volatile("s_mov_b32 m0, %0\n\ts_nop 0" ::"s"(__builtin_amdgcn_readfirstlane(v)) : "memory")
#define DMA(srd, voff, soff, imm) asm volatile("buffer_load_dwordx4 %0, %1, %2 offen offset:" #imm " lds" ::"v"(voff), "s"(srd), "s"(soff) : "memory")
#define WAITV(n) asm volatile("s_waitcnt vmcnt(" #n ")" ::: "memory")
#define PIN() __builtin_amdgcn_sched_barrier(0)
#define ORDER() asm volatile("" ::: "memory")
DI void bar() { asm volatile("s_waitcnt lgkmcnt(0)\n\ts_barrier" ::: "memory"); }
DI void bar_raw() { PIN(); asm volatile("s_barrier" ::: "memory"); PIN(); }
DI v2u trd(LAS char* p) { return __builtin_bit_cast(v2u, __builtin_amdgcn_ds_read_tr16_b64_v4i16((LAS s4*)p)); }
DI v4u trd2(LAS char* p0, LAS char* p1) { v2u a = trd(p0), b = trd(p1); return v4u{a.x, a.y, b.x, b.y}; }
DI float dot2(unsigned a, unsigned b, float c) { return __builtin_amdgcn_fdot2_f32_bf16(__builtin_bit_cast(bf2, a), __builtin_bit_cast(bf2, b), c, false); }
DI float dot8(v4u a, v4u b, float c) { return dot2(a.w, b.w, dot2(a.z, b.z, dot2(a.y, b.y, dot2(a.x, b.x, c)))); }
DI float sum8(v4u a, float c) { const unsigned o = 0x3f803f80u; return dot2(a.w, o, dot2(a.z, o, dot2(a.y, o, dot2(a.x, o, c)))); }
DI f2 fma2(f2 a, f2 b, f2 c) { return __builtin_elementwise_fma(a, b, c); }
struct cm { f2 p, n; };
DI cm mkm(f2 b) { return cm{b, f2{-b.y, b.x}}; }
DI f2 cmul(f2 a, cm b) { return fma2(a.yy, b.n, a.xx * b.p); }
DI f2 cexpl(float a, float th, float l) {
  const float m = __builtin_amdgcn_exp2f(a * l), rv = __builtin_amdgcn_fractf(th * l);
  return f2{m, m} * f2{__builtin_amdgcn_cosf(rv), __builtin_amdgcn_sinf(rv)};
}
DI float xor32(float v) { const auto r = __builtin_amdgcn_permlane32_swap(__float_as_uint(v), __float_as_uint(v), false, false); return __uint_as_float(r[0]) + __uint_as_float(r[1]); }
DI void st16(__amdgpu_buffer_rsrc_t r, v4u v, unsigned off) { __builtin_amdgcn_raw_buffer_store_b128(v, r, off, 0, 16); }
DI v4u ld16(__amdgpu_buffer_rsrc_t r, unsigned voff, unsigned soff) { return __builtin_amdgcn_raw_buffer_load_b128(r, voff, soff, 16); }
DI void put_flag(char* ws, int k, u64 tag) { __hip_atomic_store((GAS u64*)(ws + WS_FLAG + 128u * k), tag, __ATOMIC_RELAXED, __HIP_MEMORY_SCOPE_AGENT); }
DI u64 ld_word(char* ws, unsigned off) { return __hip_atomic_load((GAS u64*)(ws + off), __ATOMIC_RELAXED, __HIP_MEMORY_SCOPE_AGENT); }
DI void wait_flags(char* ws, int k, u64 tag) {
#pragma unroll 1
  for (unsigned s = 0; s < SPIN_CAP; ++s) {
    if (__all(ld_word(ws, WS_FLAG + 128u * k) == tag)) break;
    __builtin_amdgcn_s_sleep(1);
  }
  __builtin_amdgcn_fence(__ATOMIC_ACQUIRE, "wavefront");
}
DI u64 next_tag(u64 ep) { return ep * 6364136223846793005ull + 1442695040888963407ull; }
DI float ld_bf_uniform(const u16* p, int i) { const unsigned v = ((const unsigned*)p)[i >> 1]; return (i & 1) ? bfhi(v) : bflo(v); }
DI void lds_put(unsigned addr, unsigned v) { asm volatile("s_mov_b64 exec, 1\n\tds_write_b32 %0, %1\n\ts_mov_b64 exec, -1" ::"v"(addr), "v"(v) : "memory"); }
DI bool lds_arrive(LAS char* S, int c, int q) {
  asm volatile("s_waitcnt lgkmcnt(0)" ::: "memory");
  lds_put(CNT + 16 * c + 4 * q, 1u);
  const v4u v = *(volatile LAS v4u*)(S + CNT + 16 * c);
  return __builtin_amdgcn_readfirstlane(v.x & v.y & v.z & v.w);
}
DI void lds_wait(LAS char* S, int c) {
#pragma unroll 1
  for (unsigned s = 0; s < (SPIN_CAP << 4); ++s) {
    const v4u v = *(volatile LAS v4u*)(S + CNT + 16 * c);
    if (__builtin_amdgcn_readfirstlane(v.x & v.y & v.z & v.w)) break;
    __builtin_amdgcn_s_sleep(SLP);
  }
  ORDER();
}
DI void lds_set(LAS char* S, int off, unsigned v) { asm volatile("s_waitcnt lgkmcnt(0)" ::: "memory"); lds_put(off, v); }
DI unsigned lds_get(LAS char* S, unsigned off) { return *(volatile LAS unsigned*)(S + off); }
DI void lds_wait_ge(LAS char* S, unsigned off, unsigned n) {
#pragma unroll 1
  for (unsigned s = 0; s < (SPIN_CAP << 4); ++s) {
    if (__all(lds_get(S, off) >= n)) break;
    __builtin_amdgcn_s_sleep(SLP);
  }
  ORDER();
}
DI u64 get_tag(LAS char* S) { lds_wait_ge(S, TAGL + 8, 1); return *(volatile LAS u64*)(S + TAGL); }
DI u64 ld_hidden(char* ws, unsigned off) { u64 v; asm volatile("global_load_dwordx2 %0, %1, %2 sc1" : "=v"(v) : "v"(off), "s"(ws) : "memory"); return v; }
DI float sadd(float a, float b) { float r; asm("v_add_f32 %0, %1, %2" : "=v"(r) : "v"(a), "v"(b)); return r; }
DI float smul(float a, float b) { float r; asm("v_mul_f32 %0, %1, %2" : "=v"(r) : "v"(a), "v"(b)); return r; }
DI bool tag_ok(u64 g, u64 tag) { return __all((unsigned)(g >> 32) == (unsigned)tag); }
DI u64 wait_granule(char* ws, unsigned off, u64 tag) {
  u64 g = 0;
#pragma unroll 1
  for (unsigned s = 0; s < SPIN_CAP; ++s) { g = ld_word(ws, off); if (tag_ok(g, tag)) break; }
  return g;
}
#define GOT(v) asm volatile("" : "+v"(v))
DI void check_flags(char* ws, int k, u64 seen, u64 tag) {
  if (!__all(seen == tag)) wait_flags(ws, k, tag);
  __builtin_amdgcn_fence(__ATOMIC_ACQUIRE, "wavefront");
}
#define STAMP(k)
#define STAMPB(n)
#define STAMPP(n)
#define STAMPC(n)
#define STAMP_DECL
#define STAMP_OUT()

__global__ __launch_bounds__(512, 2) void s4mlp_kernel(const u16* __restrict__ x, const u16* __restrict__ Are, const u16* __restrict__ Aim,
                                                      const u16* __restrict__ Bre, const u16* __restrict__ Bim, const u16* __restrict__ Cre,
                                                      const u16* __restrict__ Cim, const u16* __restrict__ b1, const u16* __restrict__ logdt,
                                                      const u16* __restrict__ dskip, const u16* __restrict__ w1, const u16* __restrict__ w2,
                                                      const u16* __restrict__ b2, u16* __restrict__ out, char* ws) {
  LAS char* const S = (LAS char*)0;
  const int tid = threadIdx.x, lane = tid & 63, w = __builtin_amdgcn_readfirstlane(tid >> 6), wq = w & 3;
  const int b = blockIdx.x, xcd = b & 7, bi = b >> 3;
  const int rp = 4 * (xcd >> 1) + (bi >> 3), cb = 7 * (xcd & 1) + (bi & 7);
  const bool spare = (bi & 7) == 7;
  const int c0 = cb * 128 > F - 128 ? F - 128 : cb * 128;
  const int gi = rp * 14 + cb;
  const int ch0 = 20 * (gi / 7) + 3 * (gi % 7), nch = gi % 7 == 6 ? 2 : 3;
  const int rt = 8 * (xcd >> 1) + (bi >> 2), ct = 4 * (xcd & 1) + (bi & 3);
  const __amdgpu_buffer_rsrc_t rws = __builtin_amdgcn_make_buffer_rsrc(ws, 0, WS_END, 0x00020000);
  const int h = lane >> 5, g = lane >> 4, q = (lane & 15) >> 2, p = lane & 3, blk = g & 1;
  STAMP_DECL;
  STAMP(0);
  v4u pf0 = {}, pf1 = {};
  if (w == 7) {
    unsigned len;
    asm volatile("s4_code_begin:\n\ts_mov_b32 %0, s4_code_end-s4_code_begin-256" : "=s"(len));
    const v4u sc = mk_srd((const void*)__builtin_amdgcn_s_getpc(), __builtin_amdgcn_readfirstlane(len));
    asm volatile("s_nop 4\n\tbuffer_load_dwordx4 %0, %2, %3, 0 offen\n\tbuffer_load_dwordx4 %1, %4, %3, 0 offen"
                 : "=&v"(pf0), "=&v"(pf1) : "v"(bi * 1024 + lane * 16), "s"(sc), "v"(bi * 1024 + lane * 16 + 32768) : "memory");
  }
  asm volatile("" ::"s"(x), "s"(w1), "s"(ws), "s"(Are), "s"(Aim), "s"(Bre), "s"(Bim), "s"(Cre), "s"(Cim), "s"(w2), "s"(logdt), "s"(dskip));
  if (w == 7) *(LAS unsigned*)(S + CNT + 4 * lane) = 0u;
  bar();
  int sn = cb * 3 + rp * 5 + (rp >> 1);
  sn -= 10 * ((sn * 205) >> 11);
  const int s0 = sn;
  const int u0 = spare ? 448 + 3 * (xcd * 4 + (bi >> 3)) : 2 * gi;
  auto x_addr = [&](int row) { return row * 128 + 16 * (h ^ ((row >> 1) & 7)); };
  auto w_addr = [&](int c, int t2) { return 16384 + 256 * (8 * h + 4 * t2 + q) + 64 * (c ^ q) + 16 * ((2 * blk + (p >> 1)) ^ (2 * h + t2)) + 8 * (p & 1); };
  unsigned ko[7];
  int fwk;
  auto setup_k = [&]() {
    int k0 = w + 7 * (bi >> 2), ln = lane;
    asm volatile("" : "+s"(k0), "+v"(ln));
    k0 = __builtin_amdgcn_readfirstlane(k0);
#pragma unroll
    for (int j = 0; j < 7; ++j) { const int k = 8 * j + k0; ko[j] = (k >= 54 ? k - 54 : k) * 1024; }
    if (w >= 6) ko[6] = ko[0];
    int k = (ln < 14 ? 8 * (ln >> 1) : 0) + k0;
    k = k >= 54 ? k - 54 : k; k = k >= 54 ? k - 54 : k;
    fwk = FL_W + k * 10 + ((ct * 80 + (ln & 1) * 79) >> 6);
    asm volatile("" : "+v"(fwk) : "s"(ko[0]), "s"(ko[1]), "s"(ko[2]), "s"(ko[3]), "s"(ko[4]), "s"(ko[5]), "s"(ko[6]));
    PIN();
  };
  const v4u sx = mk_srd(x, L * D * 2), sw1 = mk_srd(w1, D * F * 2);
  unsigned vx[4], vw[4];
  auto stage = [&](int slot) {
    const unsigned so = sn * 128, sv = sn * (64 * F * 2);
    SET_M0(slot * SLOT + wq * 4096);
    DMA(sx, vx[0], so, 0); DMA(sx, vx[1], so, 1024); DMA(sx, vx[2], so, 2048); DMA(sx, vx[3], so, 3072);
    SET_M0(slot * SLOT + 16384 + wq * 4096);
    DMA(sw1, vw[0], sv, 0); DMA(sw1, vw[1], sv, 1024); DMA(sw1, vw[2], sv, 2048); DMA(sw1, vw[3], sv, 3072);
    sn = sn == 9 ? 0 : sn + 1;
  };
  v4u wf2[35];
  const unsigned w2v = lane * 16, w2v6 = w2v + (w < 6 ? 0u : OOB), w2s = WS_W2T + ct * (270 * 1024);
  auto load_w2 = [&](int i0, int i1) {
#pragma unroll
    for (int i = 0; i < 35; ++i)
      if (i >= i0 && i < i1) wf2[i] = ld16(rws, i >= 30 ? w2v6 : w2v, w2s + (i % 5) * (54 * 1024) + ko[i / 5]);
  };
  u64 tag, fh = 0;
  const int fhk = FL_H + (rt >> 1) * 14 + (lane < 14 ? lane : 0);
  if (w >= 4) {
    if (!spare) {
#pragma unroll
      for (int j = 0; j < 4; ++j) {
        const int rx = 32 * wq + 8 * j + (lane >> 3);
        vx[j] = (rp * 128 + rx) * (D * 2) + 16 * ((lane & 7) ^ ((rx >> 1) & 7)) - 1024 * j;
        vw[j] = (16 * wq + 4 * j + g) * (F * 2) + c0 * 2 + 16 * ((lane & 15) ^ ((g << 2) | j)) - 1024 * j;
      }
#pragma unroll 1
      for (int s = 0; s < 3; ++s) stage(s);
      setup_k();
      WAITV(16);
      GOT(pf0); GOT(pf1);
      lds_arrive(S, 1, wq);
      STAMP(1);
      WAITV(8);
      STAMP(2);
      const int t4 = tid - 256;
      const int fxk = FL_X + (lane & 15) * 10 + ((ch0 + ((lane & 16) ? nch - 1 : 0)) >> 6);
      v4u ud[3], gr[4];
      bar();
      STAMP(6);
      tag = get_tag(S);
#pragma unroll 1
      for (int n = 0; n < 6; ++n) {
        stage((n + 3) & 3);
        WAITV(8);
        bar_raw();
      }
      stage(1);
      u64 pw = ld_hidden(ws, WS_FLAG + 128u * fwk), px = ld_hidden(ws, WS_FLAG + 128u * fxk);
#pragma unroll
      for (int i = 0; i < 4; ++i) gr[i] = ld16(rws, t4 * 16, WS_ST + i * 4096);
      WAITV(14);
      bar_raw();
      STAMP(4);
      WAITV(4);
      GOT(pw); GOT(px);
      check_flags(ws, fxk, px, tag);
#pragma unroll
      for (int j = 0; j < 3; ++j) ud[j] = ld16(rws, t4 * 16, WS_XT + (ch0 + j) * 4096);
      PIN();
      {
        unsigned bad = 0;
#pragma unroll
        for (int i = 0; i < 4; ++i) {
          bad |= (gr[i].y ^ (unsigned)tag) | (gr[i].w ^ (unsigned)tag);
          *(LAS v2u*)(S + STT + i * 2048 + t4 * 8) = v2u{gr[i].x, gr[i].z};
        }
        if (!__all(bad == 0)) {
#pragma unroll 1
          for (int i = 0; i < 8; ++i) *(LAS unsigned*)(S + STT + (i >> 1) * 2048 + t4 * 8 + (i & 1) * 4) = (unsigned)wait_granule(ws, WS_ST + (i >> 1) * 4096 + t4 * 16 + (i & 1) * 8, tag);
        }
      }
      bar();
      STAMP(5);
      check_flags(ws, fwk, pw, tag);
      load_w2(0, 24);
      PIN();
      {
        const v4u sa = *(LAS v4u*)(S + STT + t4 * 32), sb = *(LAS v4u*)(S + STT + t4 * 32 + 16);
        if (t4 < 24) *(LAS v4u*)(S + UCP + (t4 >> 1) * UST + (t4 & 1) * 16) = v4u{0u, 0u, 0u, 0u};
        float rr[8];
#pragma unroll
        for (int e = 0; e < 8; ++e) rr[e] = __builtin_amdgcn_rcpf(bfhi(e < 4 ? sa[e] : sb[e - 4]));
        asm volatile("s_nop 0" : "+v"(rr[0]), "+v"(rr[1]), "+v"(rr[2]), "+v"(rr[3]), "+v"(rr[4]), "+v"(rr[5]), "+v"(rr[6]), "+v"(rr[7]));
#pragma unroll
        for (int e = 0; e < 4; ++e) {
          const float n0 = -bflo(e < 2 ? sa[2 * e] : sb[2 * e - 4]), n1 = -bflo(e < 2 ? sa[2 * e + 1] : sb[2 * e - 3]);
#pragma unroll
          for (int j = 0; j < 3; ++j) ud[j][e] = pk(smul(sadd(bflo(ud[j][e]), n0), rr[2 * e]), smul(sadd(bfhi(ud[j][e]), n1), rr[2 * e + 1]));
        }
#pragma unroll
        for (int j = 0; j < 3; ++j) *(LAS v4u*)(S + UCP + j * UCH + 32 + t4 * 16) = ud[j];
      }
      lds_arrive(S, 7, wq);
      lds_wait(S, 7);
      STAMP(12);
      {
#pragma unroll
        for (int j = 0; j < 3; ++j) {
          LAS char* U = S + UCP + j * UCH + 32 + t4 * 16;
          const v4u d = ud[j]; const v2u pv = *(LAS v2u*)(U - 8);
          const unsigned a0 = __builtin_amdgcn_alignbit(pv.y, pv.x, 16), a1 = __builtin_amdgcn_alignbit(d.x, pv.y, 16), a2 = __builtin_amdgcn_alignbit(d.y, d.x, 16),
                         a3 = __builtin_amdgcn_alignbit(d.z, d.y, 16), a4 = __builtin_amdgcn_alignbit(d.w, d.z, 16);
          *(LAS v4u*)(U + UST) = v4u{a1, a2, a3, a4};
          *(LAS v4u*)(U + 2 * UST) = v4u{pv.y, d.x, d.y, d.z};
          *(LAS v4u*)(U + 3 * UST) = v4u{a0, a1, a2, a3};
        }
      }
      lds_arrive(S, 8, wq);
      lds_wait(S, 8);
      STAMP(14);
      LAS char* Kp = S + KARR + 512 + 32 * ((lane & 15) - (g >> 1)) + 16 * blk - 64 * wq - 256;
      LAS char* Up = S + UCP + q * UST + 32 + 32 * (g >> 1) - 16 * blk + 8 * p + 64 * wq - 8;
      LAS char* Pp = S + wq * 8192 + g * 256 + (((lane & 15) ^ (2 * g)) * 16);
      f4 acc[2][8];
      v4u kf[8], kg[8], ua[2], ub[2];
      auto rd_u = [&](int j, int s) { ua[s & 1] = trd2(Up + j * UCH + 512 * s + 8, Up + j * UCH + 512 * s); ub[s & 1] = trd2(Up + j * UCH + 512 * (7 - s) + 264, Up + j * UCH + 512 * (7 - s) + 256); };
#pragma unroll
      for (int e = 0; e < 8; ++e) kf[e] = *(LAS v4u*)(Kp + 512 * e + 256);
      kg[0] = *(LAS v4u*)Kp;
      rd_u(0, 0);
      fh = ld_hidden(ws, WS_FLAG + 128u * fhk);
      lds_wait(S, 6);
      STAMP(13);
      PIN();
#pragma unroll
      for (int j = 0; j < 4; ++j) {
        if (j == 2 && nch == 2) {
#pragma unroll
          for (int R = 0; R < 8; ++R) *(LAS f4*)(Pp + SLOT + R * 1024) = acc[1][R];
          lds_arrive(S, 3, wq);
          break;
        }
        if (j == 3) {
          lds_wait(S, 3);
#pragma unroll
          for (int R = 0; R < 8; ++R) *(LAS f4*)(Pp + 2 * SLOT + R * 1024) = acc[0][R];
          lds_arrive(S, 4, wq);
          break;
        }
#pragma unroll
        for (int s = 0; s < 8; ++s) {
          if (s == 0 && j > 0) kg[0] = *(LAS v4u*)(Kp + j * KCH);
          if (s < 7) { rd_u(j, s + 1); kg[s + 1] = *(LAS v4u*)(Kp + j * KCH + 512 * (s + 1)); } else if (j < 2) rd_u(j + 1, 0);
          if (s > 0 && j < 2) kf[8 - s] = *(LAS v4u*)(Kp + (j + 1) * KCH + 512 * (8 - s) + 256);
          if (j > 0 && s >= 1 && s < 5) { *(LAS f4*)(Pp + (j - 1) * SLOT + (2 * s - 2) * 1024) = acc[(j - 1) & 1][2 * s - 2]; *(LAS f4*)(Pp + (j - 1) * SLOT + (2 * s - 1) * 1024) = acc[(j - 1) & 1][2 * s - 1]; }
#pragma unroll
          for (int R = s; R < 8; ++R) acc[j & 1][R] = __builtin_amdgcn_mfma_f32_16x16x32_bf16(asbf(ua[s & 1]), asbf(kf[R - s]), s ? acc[j & 1][R] : f4{0.f, 0.f, 0.f, 0.f}, 0, 0, 0);
#pragma unroll
          for (int R = 7 - s; R < 8; ++R) acc[j & 1][R] = __builtin_amdgcn_mfma_f32_16x16x32_bf16(asbf(ub[s & 1]), asbf(kg[R - 7 + s]), acc[j & 1][R], 0, 0, 0);
#pragma unroll
          for (int i = 0; i < 8; ++i) { __builtin_amdgcn_sched_group_barrier(0x8, 1, 0); __builtin_amdgcn_sched_group_barrier(0x80, 1, 0); }
          PIN();
          if (s == 7 && j < 2) { kf[0] = *(LAS v4u*)(Kp + (j + 1) * KCH + 256); PIN(); }
          if (j > 0 && s == 6) lds_arrive(S, 1 + j, wq);
        }
      }
      STAMP(7);
      WAITV(0);
      GOT(fh);
      if (!__all(fh == tag)) fh = ld_word(ws, WS_FLAG + 128u * fhk);
      load_w2(24, 35);
    } else {
      const int sp = xcd * 4 + (bi >> 3);
      if (sp < 27) {
        const __amdgpu_buffer_rsrc_t rw1 = __builtin_amdgcn_make_buffer_rsrc((void*)w1, 0, D * F * 2, 0x00020000);
        v4u v[20];
#pragma unroll
        for (int i = 0; i < 20; ++i) v[i] = __builtin_amdgcn_raw_buffer_load_b128(rw1, (lane >> 3) * (F * 2) + (lane & 7) * 16, (160 * wq + 8 * i) * (F * 2) + sp * 128, 0);
        PIN();
        f2 a[4] = {};
#pragma unroll
        for (int i = 0; i < 20; ++i)
#pragma unroll
          for (int e = 0; e < 4; ++e) a[e] += unpk(v[i][e]);
        *(LAS f4*)(S + (wq * 64 + lane) * 32) = f4{a[0].x, a[0].y, a[1].x, a[1].y};
        *(LAS f4*)(S + (wq * 64 + lane) * 32 + 16) = f4{a[2].x, a[2].y, a[3].x, a[3].y};
        if (lds_arrive(S, 2, wq)) {
          float t = 0.f;
#pragma unroll
          for (int i = 0; i < 32; ++i) t += *(LAS float*)(S + i * 256 + lane * 4);
          __hip_atomic_store((GAS u64*)(ws + WS_CS + (sp * 64 + lane) * 8), (get_tag(S) << 32) | __float_as_uint(t), __ATOMIC_RELAXED, __HIP_MEMORY_SCOPE_AGENT);
        }
      }
      setup_k();
      tag = get_tag(S);
      wait_flags(ws, fwk, tag);
      GOT(pf0); GOT(pf1);
      load_w2(0, 35);
    }
  } else {
    const int ch = ch0 + w;
    unsigned pr[2][6], xa[4], wa[2][2];
    v4u xv[10], wv[3][4];
    u64 ep = 0;
    if (w == 3) {
      const __amdgpu_buffer_rsrc_t rx = __builtin_amdgcn_make_buffer_rsrc((void*)x, 0, L * D * 2, 0x00020000),
                                   rw2 = __builtin_amdgcn_make_buffer_rsrc((void*)w2, 0, D * F * 2, 0x00020000);
      ep = ld_word(ws, 0);
#pragma unroll
      for (int i = 0; i < 10; ++i) xv[i] = __builtin_amdgcn_raw_buffer_load_b128(rx, (lane >> 3) * (D * 2) + (lane & 7) * 16 + i * 128, b * (8 * D * 2), 0);
#pragma unroll
      for (int k = 0; k < 3; ++k) {
        const int u = u0 + k;
        const unsigned vo = (lane >> 3) * (D * 2) + (lane & 7) * 16 + ((k < 2 || spare) && u < 540 ? 0u : OOB);
#pragma unroll
        for (int i = 0; i < 4; ++i) wv[k][i] = __builtin_amdgcn_raw_buffer_load_b128(rw2, vo, ((u / 10) * 32 + 8 * i) * (D * 2) + (u % 10) * 128, 0);
      }
    } else if (w < nch && !spare) {
#pragma unroll
      for (int rd = 0; rd < 2; ++rd) {
        const int o = ch * N + rd * 64 + lane;
        pr[rd][0] = Are[o]; pr[rd][1] = Aim[o]; pr[rd][2] = Bre[o]; pr[rd][3] = Bim[o]; pr[rd][4] = Cre[o]; pr[rd][5] = Cim[o];
      }
    }
    PIN();
    setup_k();
#pragma unroll
    for (int i = 0; i < 4; ++i) { xa[i] = x_addr(64 * (w & 1) + (lane & 31)) ^ (32 * i); wa[i >> 1][i & 1] = w_addr((w & 2) + (i >> 1), i & 1); GOT(xa[i]); GOT(wa[i >> 1][i & 1]); }
    PIN();
    if (w == 3) {
      const u64 tg = next_tag(ep);
      lds_put(TAGL, (unsigned)tg); lds_put(TAGL + 4, (unsigned)(tg >> 32));
      lds_set(S, TAGL + 8, 1);
      float sm[2] = {}, sq[2] = {};
#pragma unroll
      for (int i = 0; i < 10; ++i) { sm[i & 1] = sum8(xv[i], sm[i & 1]); sq[i & 1] = dot8(xv[i], xv[i], sq[i & 1]); }
      float sum = sm[0] + sm[1], ssq = sq[0] + sq[1];
#pragma unroll
      for (int m = 1; m < 8; m <<= 1) { sum += __shfl_xor(sum, m); ssq += __shfl_xor(ssq, m); }
      const float mean = sum * (1.f / D), var = fmaxf(ssq * (1.f / D) - mean * mean, 0.f);
      const unsigned ms = pk(mean, __builtin_amdgcn_sqrtf(rbf(rbf(var) + 1e-5f)));
      if ((lane & 7) == 0) __hip_atomic_store((GAS u64*)(ws + WS_ST + (b * 8 + (lane >> 3)) * 8), (tg << 32) | ms, __ATOMIC_RELAXED, __HIP_MEMORY_SCOPE_AGENT);
      STAMP(1);
      LAS char* B = S + W2U + (4 * (blk + 4 * (g >> 1)) + q) * 128 + 8 * p;
      GOT(wv[2][0]); GOT(wv[2][1]); GOT(wv[2][2]); GOT(wv[2][3]);
#pragma unroll
      for (int k = 0; k < 3; ++k) {
        const int u = u0 + k;
        if ((k < 2 || spare) && u < 540) {
#pragma unroll
          for (int i = 0; i < 4; ++i) *(LAS v4u*)(S + W2U + i * 1024 + lane * 16) = wv[k][i];
#pragma unroll
          for (int n = 0; n < 4; ++n) st16(rws, trd2(B + 32 * n, B + 32 * n + 1024), lane * 16 + WS_W2T + (((u % 10) * 4 + n) * 54 + u / 10) * 1024);
        }
      }
      STAMP(11);
      if (cb < 10 && !spare) {
        lds_wait(S, 1);
        STAMP(12);
#pragma unroll 1
        for (int c = 0; c < 4; ++c) {
          v2u xt[8];
#pragma unroll
          for (int m = 0; m < 8; ++m) {
            const int rr = 32 * g + 4 * m + q;
            xt[m] = trd(S + 128 * rr + 16 * ((2 * c + (p >> 1)) ^ ((rr >> 1) & 7)) + 8 * (p & 1));
          }
#pragma unroll
          for (int k = 0; k < 4; ++k) *(LAS v4u*)(S + W2U + (lane & 15) * 272 + g * 64 + k * 16) = v4u{xt[2 * k].x, xt[2 * k].y, xt[2 * k + 1].x, xt[2 * k + 1].y};
#pragma unroll
          for (int k = 0; k < 4; ++k)
            st16(rws, *(LAS v4u*)(S + W2U + (4 * k + g) * 272 + (lane & 15) * 16), WS_XT + (s0 * 64 + 16 * c + 4 * k + g) * 4096 + rp * 256 + (lane & 15) * 16);
        }
      }
    } else if (w < nch && !spare) {
      const float dt = rbf(__expf(ld_bf_uniform(logdt, ch))), dsk = ld_bf_uniform(dskip, ch);
      f4 ka[3] = {};
      LAS unsigned* T = (LAS unsigned*)(S + TAB + w * TCH) + lane;
      LAS char* Tr = S + TAB + w * TCH + g * 16;
      constexpr int R = TROW / 4;
#pragma unroll 1
      for (int rd = 0; rd < 2; ++rd) {
        const float a = dt * bflo(pr[0][0]) * 1.4426950408889634f;
        float th = dt * bflo(pr[0][1]) * 0.15915494309189535f;
        th -= __builtin_rintf(th);
        const f2 e1 = cexpl(a, th, 1.f), e128 = cexpl(a, th, 128.f);
        const cm m1 = mkm(e1), m128 = mkm(e128), m8 = mkm(cexpl(a, -th, 8.f));
        const f2 e2 = cmul(e1, m1);
        const cm m2 = mkm(e2), m4 = mkm(cmul(e2, m2));
        f2 y = cmul(f2{bflo(pr[0][4]), -bflo(pr[0][5])}, mkm(f2{dt, dt} * f2{bflo(pr[0][2]), -bflo(pr[0][3])}));
        f2 u = {1.f, 0.f}, v = e128, t = cexpl(a, th, 1024.f), z = cmul(e128, m128);
#pragma unroll
        for (int i = 0; i < 8; ++i) {
          T[i * R] = pk2(u); T[(8 + i) * R] = pk2(v); T[(32 + i) * R] = pk2(t);
          T[(41 + 2 * i) * R] = pk2(y); y = cmul(y, m8); T[(42 + 2 * i) * R] = pk2(y);
          if (i < 7) { u = cmul(u, m1); v = cmul(v, m1); y = cmul(y, m8); }
          t = cmul(t, m128);
          if (i < 2) {
            const f2 z2 = cmul(z, m2);
            T[(16 + 4 * i) * R] = pk2(z); T[(17 + 4 * i) * R] = pk2(z2); T[(18 + 4 * i) * R] = pk2(cmul(z, m4)); T[(19 + 4 * i) * R] = pk2(cmul(z2, m4));
          } else if (i < 6) {
            T[(20 + 2 * i) * R] = pk2(z); T[(21 + 2 * i) * R] = pk2(cmul(z, m4));
          }
          if (i < 5) z = cmul(z, m128);
        }
        T[40 * R] = pk2(t);
        const int r = lane & 15, r2 = r > 8 ? 8 : r;
        v4u fa[4][4];
#pragma unroll
        for (int ks = 0; ks < 4; ++ks) {
          fa[ks][3] = *(LAS v4u*)(Tr + (41 + r) * TROW + ks * 64); fa[ks][0] = *(LAS v4u*)(Tr + r * TROW + ks * 64);
          fa[ks][1] = *(LAS v4u*)(Tr + (16 + r) * TROW + ks * 64); fa[ks][2] = *(LAS v4u*)(Tr + (32 + r2) * TROW + ks * 64);
        }
        PIN();
#pragma unroll
        for (int ks = 0; ks < 4; ++ks)
#pragma unroll
          for (int i = 0; i < 3; ++i) ka[i] = __builtin_amdgcn_mfma_f32_16x16x32_bf16(asbf(fa[ks][i]), asbf(fa[ks][3]), ka[i], 0, 0, 0);
#pragma unroll
        for (int i = 0; i < 6; ++i) pr[0][i] = pr[1][i];
      }
      STAMP(1);
      const int c = lane & 15;
      LAS char* K = S + KARR + w * KCH;
      LAS u16* T1 = (LAS u16*)(S + TAB + w * TCH);
      if (lane == 0) ka[0][0] += dsk;
      *(LAS v2u*)(K + 512 + 256 * (g >> 1) + 16 * c + 8 * blk) = v2u{pk2(ka[0].xy), pk2(ka[0].zw)};
      const unsigned p01 = pk2(ka[1].xy), p23 = pk2(ka[1].zw), q01 = pk2(ka[2].xy), q23 = pk2(ka[2].zw);
      if (g < 2) {
        *(LAS v2u*)(T1 + 64 * g + 4 * c) = v2u{p01, p23};
        LAS u16* T3 = T1 + 264 + 64 * g + c;
        T3[0] = (u16)q01; T3[16] = (u16)(q01 >> 16); T3[32] = (u16)q23; T3[48] = (u16)(q23 >> 16);
        if (lane == 0) T1[132 + 128] = (u16)q01;
      } else {
        *(LAS unsigned*)(T1 + 132 + 64 * (g - 2) + 2 * c) = p01;
        *(LAS unsigned*)(T1 + 132 + 64 * (g - 2) + 32 + 2 * c) = p23;
        if (lane == 32) { T1[128] = (u16)p01; T1[264 + 128] = (u16)q01; }
      }
      ORDER();
      const v2u a = *(LAS v2u*)(T1 + 4 * (lane & 31));
      const unsigned a4 = T1[4 * (lane & 31) + 4], b0 = *(LAS unsigned*)(T1 + 132 + 2 * lane), b2_ = T1[132 + 2 * lane + 2];
      const unsigned c0_ = T1[264 + lane], c1 = T1[265 + lane], d0 = T1[328 + lane], d1 = T1[329 + lane];
      v4u o = {(a.x & 0xffffu) * 0x10001u, (a.x >> 16) | (a.y << 16), (a.y & 0xffffu) * 0x10001u, (a.y >> 16) | (a4 << 16)};
      if (h) o = v4u{0u, 0u, 0u, 0u};
      *(LAS v4u*)(K + (h ? 0 : 1024) + 16 * (lane & 31)) = o;
      *(LAS v4u*)(K + 1536 + 16 * lane) = v4u{(b0 & 0xffffu) * 0x10001u, b0, (b0 >> 16) * 0x10001u, b2_ * 0x10001u};
      *(LAS v4u*)(K + 2560 + 16 * lane) = v4u{c0_ * 0x10001u, c0_ * 0x10001u, ((lane & 1) ? c1 : c0_) | (c1 << 16), c1 * 0x10001u};
      *(LAS v4u*)(K + 3584 + 16 * lane) = v4u{d0 * 0x10001u, d0 * 0x10001u, ((lane & 1) ? d1 : d0) | (d1 << 16), d1 * 0x10001u};
    }
    STAMP(2);
    if (!spare) {
      const int wf = w >> 1, tg = w & 1;
      f16v acc[2][2] = {};
      v4u fx[4][2], fw[4][2];
      auto rd = [&](int set, int so, int ks) {
        fw[set][0] = trd2(S + wa[0][0] + so + 4096 * ks, S + wa[0][1] + so + 4096 * ks);
        fx[set][0] = *(LAS v4u*)(S + xa[ks] + so);
        fx[set][1] = *(LAS v4u*)(S + xa[ks] + so + 4096);
        fw[set][1] = trd2(S + wa[1][0] + so + 4096 * ks, S + wa[1][1] + so + 4096 * ks);
      };
      auto mm = [&](int set) {
#pragma unroll
        for (int i = 0; i < 4; ++i) acc[i >> 1][i & 1] = __builtin_amdgcn_mfma_f32_32x32x16_bf16(asbf(fw[set][i >> 1]), asbf(fx[set][i & 1]), acc[i >> 1][i & 1], 0, 0, 0);
      };
      auto mix = [&]() {
#pragma unroll
        for (int i = 0; i < 3; ++i) { __builtin_amdgcn_sched_group_barrier(0x8, 1, 0); __builtin_amdgcn_sched_group_barrier(0x100, 2, 0); }
        __builtin_amdgcn_sched_group_barrier(0x8, 1, 0);
        PIN();
      };
      u64 fw_seen = 0, g0 = 0, g1 = 0, gc = 0;
      const unsigned o0 = WS_ST + (rp * 128 + 64 * tg + (lane & 31)) * 8, oc = WS_CS + (c0 + 64 * wf + lane) * 8;
      unsigned b1v = 0;
      asm volatile("" : "+v"(acc[0][0]), "+v"(acc[0][1]), "+v"(acc[1][0]), "+v"(acc[1][1]));
      bar();
      STAMP(6);
      rd(0, 0, 0); rd(1, 0, 1);
      PIN();
#pragma unroll 1
      for (int n = 0; n < 5; ++n) {
        rd(2, 0, 2); mm(0); mix();
        rd(3, 0, 3); mm(1); mix();
        rd(0, SLOT, 0); mm(2); mix();
        rd(1, SLOT, 1); mm(3); mix();
        if (n == 4) { fw_seen = ld_hidden(ws, WS_FLAG + 128u * fwk); g0 = ld_hidden(ws, o0); g1 = ld_hidden(ws, o0 + 256); gc = ld_hidden(ws, oc);
          asm volatile("global_load_ushort %0, %1, %2" : "=v"(b1v) : "v"((c0 + 64 * wf + lane) * 2), "s"(b1) : "memory"); }
        else bar_raw();
        rd(2, SLOT, 2); mm(0); mix();
        rd(3, SLOT, 3); mm(1); mix();
#pragma unroll
        for (int i = 0; i < 4; ++i) { xa[i] ^= 2 * SLOT; wa[i >> 1][i & 1] ^= 2 * SLOT; }
        rd(0, 0, 0); mm(2); mix();
        rd(1, 0, 1); mm(3); mix();
        if (n == 1 && w == 3) {
          WAITV(0);
          if (lane < (cb < 10 ? 3 : 2)) put_flag(ws, lane < 2 ? FL_W + u0 + lane : FL_X + rp * 10 + s0, *(volatile LAS u64*)(S + TAGL));
        }
        if (n < 4) bar_raw();
      }
      STAMP(3);
      tag = get_tag(S);
      WAITV(0); GOT(fw_seen); GOT(g0); GOT(g1); GOT(gc); GOT(b1v);
      *(LAS float*)(S + COLT + w * 512 + 256 + lane * 4) = bflo(b1v);
      if (!tag_ok(gc, tag)) gc = wait_granule(ws, oc, tag);
      *(LAS unsigned*)(S + COLT + w * 512 + lane * 4) = (unsigned)gc;
      if (!tag_ok(g0, tag)) g0 = wait_granule(ws, o0, tag);
      if (!tag_ok(g1, tag)) g1 = wait_granule(ws, o0 + 256, tag);
      check_flags(ws, fwk, fw_seen, tag);
      const unsigned hv = WS_HT + (((rp * 8 + 4 * tg + blk) * 54 + (c0 >> 5) + 2 * wf) * 64 + (lane & 15) + 16 * h) * 16 + ((cb == 13 && wf == 0) ? OOB : 0u);
#pragma unroll
      for (int tt = 0; tt < 2; ++tt) {
        const float rs = __builtin_amdgcn_rcpf(bfhi((unsigned)(tt ? g1 : g0))), mr = -bflo((unsigned)(tt ? g1 : g0)) * rs;
#pragma unroll
        for (int c = 0; c < 2; ++c) {
          f4 cs[4], bb[4];
#pragma unroll
          for (int a = 0; a < 4; ++a) { LAS char* ct_ = S + COLT + w * 512 + (32 * c + 8 * a + 4 * h) * 4; cs[a] = *(LAS f4*)ct_; bb[a] = *(LAS f4*)(ct_ + 256); }
          unsigned o[8];
#pragma unroll
          for (int a = 0; a < 4; ++a) {
#pragma unroll
            for (int e = 0; e < 2; ++e) {
              const f2 v = fma2(f2{acc[c][tt][4 * a + 2 * e], acc[c][tt][4 * a + 2 * e + 1]}, f2{rs, rs}, fma2(f2{mr, mr}, e ? cs[a].zw : cs[a].xy, e ? bb[a].zw : bb[a].xy));
              const f2 z = v * fma2(v * v, f2{-0.10294324f, -0.10294324f}, f2{-2.30220819f, -2.30220819f});
              const f2 dn = f2{__builtin_amdgcn_exp2f(z.x), __builtin_amdgcn_exp2f(z.y)} + f2{1.f, 1.f};
              o[2 * a + e] = pk2(v * f2{__builtin_amdgcn_rcpf(dn.x), __builtin_amdgcn_rcpf(dn.y)});
            }
            if (a & 1) { GOT(o[2 * a + 1]); load_w2(2 * (2 * tt + c) + (a >> 1), 2 * (2 * tt + c) + (a >> 1) + 1); PIN(); }
          }
          st16(rws, v4u{o[0], o[1], o[2], o[3]}, hv + (tt * 108 + c) * 1024);
          st16(rws, v4u{o[4], o[5], o[6], o[7]}, hv + (tt * 108 + c) * 1024 + 512);
          PIN();
        }
      }
      STAMP(4);
      lds_arrive(S, 6, wq);
      load_w2(8, 16);
      WAITV(8);
      if (lds_arrive(S, 0, wq) && lane == 0) put_flag(ws, FL_H + gi, tag);
      STAMP(5);
      load_w2(16, 35);
      fh = ld_hidden(ws, WS_FLAG + 128u * fhk);
#pragma unroll 1
      for (int j = 0; j < nch; ++j) {
        lds_wait(S, 2 + j);
        const unsigned pa = j * SLOT + (tid >> 5) * 1024 + (((tid >> 1) & 15) ^ (4 * (tid & 1))) * 16 + (tid & 1) * 512;
        f4 lo[4], hi[4];
#pragma unroll
        for (int v = 0; v < 4; ++v) { lo[v] = *(LAS f4*)(S + pa + v * 8192); hi[v] = *(LAS f4*)(S + (pa ^ 32u) + 256 + v * 8192); }
        f4 l, m;
#pragma unroll
        for (int e = 0; e < 4; ++e) { l[e] = sadd(sadd(lo[0][e], lo[1][e]), sadd(lo[2][e], lo[3][e])); m[e] = sadd(sadd(hi[0][e], hi[1][e]), sadd(hi[2][e], hi[3][e])); }
        st16(rws, v4u{pk2(l.xy), pk2(l.zw), pk2(m.xy), pk2(m.zw)}, tid * 16 + WS_YT + (ch0 + j) * 4096);
      }
      STAMP(7);
      if (nch == 3) WAITV(3); else WAITV(2);
    } else {
      tag = get_tag(S);
      if (w == 3) { WAITV(0); if (lane < 3 && u0 + lane < 540) put_flag(ws, FL_W + u0 + lane, tag); }
      wait_flags(ws, fwk, tag);
      load_w2(0, 35);
    }
  }
  {
    const int et = 2 * w + (lane >= 20), eq = lane < 20 ? lane : lane < 40 ? lane - 20 : 0;
    GOT(fh);
    check_flags(ws, fhk, fh, tag);
    const v2u bb2 = *(const v2u*)(b2 + ct * 80 + 4 * eq);
    STAMP(8);
    constexpr int HR = 7;
    v4u hf[HR];
    unsigned hb[4];
#pragma unroll
    for (int pt = 0; pt < 4; ++pt) hb[pt] = WS_HT + (rt * 4 + ((pt + ct) & 3)) * (54 * 1024);
    auto load_h = [&](int i) { hf[i % HR] = ld16(rws, w2v, hb[i / 7] + ko[i % 7]); };
#pragma unroll
    for (int i = 0; i < HR; ++i) load_h(i);
    PIN();
    if (w < 4 && !spare) {
      WAITV(8);
      if (lds_arrive(S, 5, wq) && lane == 0) put_flag(ws, FL_Y + gi, tag);
    }
    bar();
    STAMP(9);
    LAS char* Pw = S + w * GW + g * 256 + (((lane & 15) + g) & 15) * 16;
    LAS char* Pr = S + (eq >> 2) * GT + (eq & 3) * 256 + ((et + (eq & 3)) & 15) * 16;
    const __amdgpu_buffer_rsrc_t rx = __builtin_amdgcn_make_buffer_rsrc((void*)x, 0, L * D * 2, 0x00020000),
                                 ro = __builtin_amdgcn_make_buffer_rsrc(out, 0, L * D * 2, 0x00020000);
    const unsigned ov = (et * D + 4 * eq) * 2 + (lane < 40 ? 0u : OOB), ob = (rt * 64 * D + ct * 80) * 2;
    LAS char* Yr = S + YT2 + et * YT2ROW + 8 * eq;
    const int fyk = FL_Y + ct * 28 + (lane < 28 ? lane : 0);
    f4 acc[2][5];
    v2u yb[4], xr[4];
    v4u yv[2];
#pragma unroll
    for (int pt = 0; pt < 6; ++pt) {
      f4 pr[4], sa;
      auto add4 = [](f4 a, f4 c) { return f4{sadd(a.x, c.x), sadd(a.y, c.y), sadd(a.z, c.z), sadd(a.w, c.w)}; };
#pragma unroll
      for (int j = 0; j < 7; ++j) {
        if (pt >= 2 && j == 2) { sa = add4(add4(pr[0], pr[1]), add4(pr[2], pr[3])); GOT(sa); }
        if (pt >= 2 && j == 4) { sa = add4(add4(sa, pr[0]), pr[1]); GOT(sa); }
        if (pt >= 2 && j == 5) { sa = add4(add4(sa, pr[2]), pr[3]); GOT(sa); }
        if (pt >= 2 && (j == 0 || j == 2)) {
#pragma unroll
          for (int v = 0; v < 4; ++v) pr[v] = *(LAS f4*)(Pr + (pt & 1) * GBUF + (2 * j + v) * GW);
        }
        if (j >= 1 && j < 6 && pt >= 1 && pt < 5) *(LAS f4*)(Pw + ((pt - 1) & 1) * GBUF + (j - 1) * GT) = acc[(pt - 1) & 1][j - 1];
        if (pt >= 2 && j == 6) {
          const unsigned m0 = pk2(sa.xy), m1 = pk2(sa.zw);
          yb[pt - 2] = v2u{pk(sadd(bflo(m0), bflo(bb2.x)), sadd(bfhi(m0), bfhi(bb2.x))), pk(sadd(bflo(m1), bflo(bb2.y)), sadd(bfhi(m1), bfhi(bb2.y)))};
          GOT(yb[pt - 2]);
        }
        if (pt < 4) {
#pragma unroll
          for (int n = 0; n < 5; ++n)
            acc[pt & 1][n] = __builtin_amdgcn_mfma_f32_16x16x32_bf16(asbf(wf2[5 * j + n]), asbf(hf[(7 * pt + j) % HR]), j ? acc[pt & 1][n] : f4{0.f, 0.f, 0.f, 0.f}, 0, 0, 0);
          if (7 * pt + j + HR < 28) load_h(7 * pt + j + HR);
        }
        PIN();
      }
      STAMPP(pt);
      if (pt == 1) fh = ld_hidden(ws, WS_FLAG + 128u * fyk);
      if (pt == 2) {
        WAITV(7);
        GOT(fh);
        check_flags(ws, fyk, fh, tag);
#pragma unroll
        for (int j = 0; j < 2; ++j) {
          const int c = lane + 64 * j;
          yv[j] = ld16(rws, (10 * w + (c >> 3)) * 4096 + (c & 7) * 16 + (c < 80 ? 0u : OOB), WS_YT + ct * 80 * 4096 + rt * 128);
        }
      }
      if (pt == 3) {
#pragma unroll
        for (int k = 0; k < 4; ++k) xr[k] = __builtin_amdgcn_raw_buffer_load_b64(rx, ov, ob + ((k + ct) & 3) * (16 * D * 2), 0);
      }
      if (pt == 4) {
#pragma unroll
        for (int j = 0; j < 2; ++j) {
          const int c = lane + 64 * j;
          LAS u16* d = (LAS u16*)(S + YT2 + (c & 7) * 8 * YT2ROW) + 10 * w + (c >> 3);
          if (c < 80) {
#pragma unroll
            for (int e = 0; e < 4; ++e) { d[(2 * e) * (YT2ROW / 2)] = (u16)yv[j][e]; d[(2 * e + 1) * (YT2ROW / 2)] = (u16)(yv[j][e] >> 16); }
          }
        }
      }
      if (pt < 5) bar();
    }
#pragma unroll
    for (int pt = 0; pt < 4; ++pt) {
      const int t0 = ((pt + ct) & 3) * 16;
      const v2u y = *(LAS v2u*)(Yr + t0 * YT2ROW);
      __builtin_amdgcn_raw_buffer_store_b64(
          v2u{pk2(unpk(pk2(unpk(xr[pt].x) + unpk(y.x))) + unpk(yb[pt].x)), pk2(unpk(pk2(unpk(xr[pt].y) + unpk(y.y))) + unpk(yb[pt].y))}, ro, ov, ob + t0 * (D * 2), 0);
    }
  }
  STAMP(10);
  STAMP_OUT();
  if (b == 0 && w == 7) {
#pragma unroll 1
    for (unsigned s = 0, j = 0; s < SPIN_CAP && j < 4; ++s)
      if (__all((unsigned)(ld_word(ws, WS_ST + (64 * j + lane) * 64) >> 32) == (unsigned)tag)) ++j;
    if (lane == 0) __hip_atomic_store((GAS u64*)ws, tag, __ATOMIC_RELAXED, __HIP_MEMORY_SCOPE_AGENT);
  }
  asm volatile("s4_code_end:");
}
}

extern "C" void kernel_launch(void* const* d_in, const int* in_sizes, int n_in, void* d_out, int out_size, void* d_ws, size_t ws_size,
                              hipStream_t stream) {
  (void)in_sizes; (void)n_in; (void)out_size; (void)ws_size;
  (void)hipFuncSetAttribute((const void*)s4mlp_kernel, hipFuncAttributeMaxDynamicSharedMemorySize, LDS_BYTES);
  s4mlp_kernel<<<256, 512, LDS_BYTES, stream>>>((const u16*)d_in[0], (const u16*)d_in[1], (const u16*)d_in[2], (const u16*)d_in[3], (const u16*)d_in[4],
                                                (const u16*)d_in[5], (const u16*)d_in[6], (const u16*)d_in[11], (const u16*)d_in[7], (const u16*)d_in[8],
                                                (const u16*)d_in[10], (const u16*)d_in[12], (const u16*)d_in[13], (u16*)d_out, (char*)d_ws);
}
```

```cpp
#include <hip/hip_runtime.h>
#include <hip/hip_bf16.h>
#include <stdint.h>

namespace {
typedef unsigned short u16;
typedef unsigned long long u64;
typedef __bf16 bf8 __attribute__((ext_vector_type(8)));
typedef __bf16 bf2 __attribute__((ext_vector_type(2)));
typedef short s4 __attribute__((ext_vector_type(4)));
typedef float f16v __attribute__((ext_vector_type(16)));
typedef float f4 __attribute__((ext_vector_type(4)));
typedef float f2 __attribute__((ext_vector_type(2)));
typedef unsigned v4u __attribute__((ext_vector_type(4)));
typedef unsigned v2u __attribute__((ext_vector_type(2)));
#define LAS __attribute__((address_space(3)))
#define GAS __attribute__((address_space(1)))
#define DI __device__ __forceinline__

constexpr int L = 2048, D = 640, N = 128, F = 1728;
constexpr unsigned WS_FLAG = 4096;
constexpr int FL_H = 0, FL_X = 224, FL_Y = 384, FL_W = 640;
constexpr unsigned WS_ST = 256u << 10;
constexpr unsigned WS_CS = WS_ST + 2048 * 8;
constexpr unsigned WS_HT = 1u << 20;
constexpr unsigned WS_W2T = WS_HT + 128u * 54 * 1024;
constexpr unsigned WS_XT = WS_W2T + 40u * 54 * 1024;
constexpr unsigned WS_YT = WS_XT + 640u * 4096;
constexpr unsigned WS_END = WS_YT + 640u * 4096;
constexpr unsigned WS_DBG = 32u << 20;
constexpr unsigned OOB = 0x40000000u;
constexpr int LDS_BYTES = 163840;
constexpr int SLOT = 32768;
constexpr int TAB = 98304, TROW = 272, TW = 32 * TROW;
constexpr int CNT = TAB + 4 * TW, TAGL = CNT + 240;
constexpr int COLT = CNT + 256;
constexpr int W2U_G = COLT + 2048, W2U_S = 16384;
constexpr int KCH = 4608, KARR = LDS_BYTES - 6 * KCH;
constexpr int UCP = 65536, UST = 4160, UCH = 4 * UST;
constexpr int GT = 1088, GW = 5 * GT, GBUF = 8 * GW, YT2 = CNT + 256, YT2ROW = 168;
static_assert(W2U_G + 4096 <= KARR + 4 * KCH && UCP + 4 * UCH <= CNT && 3 * GBUF <= CNT && YT2 + 64 * YT2ROW <= LDS_BYTES, "LDS map");
#define NB1 24
#define WARM 0
#define LAUX 16
constexpr unsigned SPIN_CAP = 1u << 17;
#define SLP 1

DI float bflo(unsigned v) { return __uint_as_float(v << 16); }
DI float bfhi(unsigned v) { return __uint_as_float(v & 0xffff0000u); }
DI f2 unpk(unsigned v) { return f2{bflo(v), bfhi(v)}; }
DI unsigned pk2(f2 p) { return __builtin_bit_cast(unsigned, __builtin_convertvector(p, bf2)); }
DI unsigned pk(float a, float b) { return pk2(f2{a, b}); }
DI float rbf(float a) { return bflo(pk(a, a)); }
DI bf8 asbf(v4u v) { return __builtin_bit_cast(bf8, v); }
DI v4u mk_srd(const void* p, unsigned bytes) { u64 a = (u64)p; return v4u{(unsigned)a, (unsigned)(a >> 32) & 0xffffu, bytes, 0x00020000u}; }
#define SET_M0(v) asm volatile("s_mov_b32 m0, %0\n\ts_nop 0" ::"s"(__builtin_amdgcn_readfirstlane(v)) : "memory")
#define DMA(srd, voff, soff, imm) asm volatile("buffer_load_dwordx4 %0, %1, %2 offen offset:" #imm " lds" ::"v"(voff), "s"(srd), "s"(soff) : "memory")
#define WAITV(n) asm volatile("s_waitcnt vmcnt(" #n ")" ::: "memory")
#define PIN() __builtin_amdgcn_sched_barrier(0)
#define ORDER() asm volatile("" ::: "memory")
DI void bar() { asm volatile("s_waitcnt lgkmcnt(0)\n\ts_barrier" ::: "memory"); }
DI void bar_raw() { PIN(); asm volatile("s_barrier" ::: "memory"); PIN(); }
DI v2u trd(LAS char* p) { return __builtin_bit_cast(v2u, __builtin_amdgcn_ds_read_tr16_b64_v4i16((LAS s4*)p)); }
DI v4u trd2(LAS char* p0, LAS char* p1) { v2u a = trd(p0), b = trd(p1); return v4u{a.x, a.y, b.x, b.y}; }
DI float dot2(unsigned a, unsigned b, float c) { return __builtin_amdgcn_fdot2_f32_bf16(__builtin_bit_cast(bf2, a), __builtin_bit_cast(bf2, b), c, false); }
DI float dot8(v4u a, v4u b, float c) { return dot2(a.w, b.w, dot2(a.z, b.z, dot2(a.y, b.y, dot2(a.x, b.x, c)))); }
DI float sum8(v4u a, float c) { const unsigned o = 0x3f803f80u; return dot2(a.w, o, dot2(a.z, o, dot2(a.y, o, dot2(a.x, o, c)))); }
DI f2 fma2(f2 a, f2 b, f2 c) { return __builtin_elementwise_fma(a, b, c); }
struct cm { f2 p, n; };
DI cm mkm(f2 b) { return cm{b, f2{-b.y, b.x}}; }
DI f2 cmul(f2 a, cm b) { return fma2(a.yy, b.n, a.xx * b.p); }
DI f2 cexpl(float a, float th, float l) {
  const float m = __builtin_amdgcn_exp2f(a * l), rv = __builtin_amdgcn_fractf(th * l);
  return f2{m, m} * f2{__builtin_amdgcn_cosf(rv), __builtin_amdgcn_sinf(rv)};
}
DI float xor32(float v) { const auto r = __builtin_amdgcn_permlane32_swap(__float_as_uint(v), __float_as_uint(v), false, false); return __uint_as_float(r[0]) + __uint_as_float(r[1]); }
DI void st16(__amdgpu_buffer_rsrc_t r, v4u v, unsigned off) { __builtin_amdgcn_raw_buffer_store_b128(v, r, off, 0, 16); }
DI v4u ld16(__amdgpu_buffer_rsrc_t r, unsigned voff, unsigned soff) { return __builtin_amdgcn_raw_buffer_load_b128(r, voff, soff, LAUX); }
DI void put_flag(char* ws, int k, u64 tag) { __hip_atomic_store((GAS u64*)(ws + WS_FLAG + 128u * k), tag, __ATOMIC_RELAXED, __HIP_MEMORY_SCOPE_AGENT); }
DI u64 ld_word(char* ws, unsigned off) { return __hip_atomic_load((GAS u64*)(ws + off), __ATOMIC_RELAXED, __HIP_MEMORY_SCOPE_AGENT); }
DI void wait_flags(char* ws, int k, u64 tag) {
#pragma unroll 1
  for (unsigned s = 0; s < SPIN_CAP; ++s) {
    if (__all(ld_word(ws, WS_FLAG + 128u * k) == tag)) break;
    __builtin_amdgcn_s_sleep(1);
  }
  __builtin_amdgcn_fence(__ATOMIC_ACQUIRE, "wavefront");
}
DI u64 next_tag(u64 ep) { return ep * 6364136223846793005ull + 1442695040888963407ull; }
DI float ld_bf_uniform(const u16* p, int i) { const unsigned v = ((const unsigned*)p)[i >> 1]; return (i & 1) ? bfhi(v) : bflo(v); }
DI void lds_put(unsigned addr, unsigned v) { asm volatile("s_mov_b64 exec, 1\n\tds_write_b32 %0, %1\n\ts_mov_b64 exec, -1" ::"v"(addr), "v"(v) : "memory"); }
DI bool lds_arrive(LAS char* S, int c, int q) {
  asm volatile("s_waitcnt lgkmcnt(0)" ::: "memory");
  lds_put(CNT + 16 * c + 4 * q, 1u);
  const v4u v = *(volatile LAS v4u*)(S + CNT + 16 * c);
  return __builtin_amdgcn_readfirstlane(v.x & v.y & v.z & v.w);
}
DI void lds_wait(LAS char* S, int c) {
#pragma unroll 1
  for (unsigned s = 0; s < (SPIN_CAP << 4); ++s) {
    const v4u v = *(volatile LAS v4u*)(S + CNT + 16 * c);
    if (__builtin_amdgcn_readfirstlane(v.x & v.y & v.z & v.w)) break;
    __builtin_amdgcn_s_sleep(SLP);
  }
  ORDER();
}
DI void lds_set(LAS char* S, int off, unsigned v) { asm volatile("s_waitcnt lgkmcnt(0)" ::: "memory"); lds_put(off, v); }
DI unsigned lds_get(LAS char* S, unsigned off) { return *(volatile LAS unsigned*)(S + off); }
DI void lds_wait_ge(LAS char* S, unsigned off, unsigned n) {
#pragma unroll 1
  for (unsigned s = 0; s < (SPIN_CAP << 4); ++s) {
    if (__all(lds_get(S, off) >= n)) break;
    __builtin_amdgcn_s_sleep(SLP);
  }
  ORDER();
}
DI u64 rd_tag(LAS char* S) { const v2u t = *(volatile LAS v2u*)(S + TAGL); return ((u64)(unsigned)__builtin_amdgcn_readfirstlane(t.y) << 32) | (unsigned)__builtin_amdgcn_readfirstlane(t.x); }
DI u64 get_tag(LAS char* S) { lds_wait_ge(S, TAGL + 8, 1); return rd_tag(S); }
DI u64 ld_hidden(char* ws, unsigned off) { u64 v; asm volatile("global_load_dwordx2 %0, %1, %2 sc1" : "=v"(v) : "v"(off), "s"(ws) : "memory"); return v; }
DI float sadd(float a, float b) { float r; asm("v_add_f32 %0, %1, %2" : "=v"(r) : "v"(a), "v"(b)); return r; }
DI float smul(float a, float b) { float r; asm("v_mul_f32 %0, %1, %2" : "=v"(r) : "v"(a), "v"(b)); return r; }
DI float ssub(float a, float b) { float r; asm("v_sub_f32 %0, %1, %2" : "=v"(r) : "v"(a), "v"(b)); return r; }
DI bool tag_ok(u64 g, u64 tag) { return __all((unsigned)(g >> 32) == (unsigned)tag); }
DI u64 wait_granule(char* ws, unsigned off, u64 tag) {
  u64 g = 0;
#pragma unroll 1
  for (unsigned s = 0; s < SPIN_CAP; ++s) { g = ld_word(ws, off); if (tag_ok(g, tag)) break; }
  return g;
}
#define GOT(v) asm volatile("" : "+v"(v))
DI void check_flags(char* ws, int k, u64 seen, u64 tag) {
  if (!__all(seen == tag)) wait_flags(ws, k, tag);
  __builtin_amdgcn_fence(__ATOMIC_ACQUIRE, "wavefront");
}
#define STAMP(k)
#define STAMP_DECL
#define STAMP_OUT()

__global__ __launch_bounds__(512, 2) void s4mlp_kernel(const u16* __restrict__ x, const u16* __restrict__ Are, const u16* __restrict__ Aim,
                                                      const u16* __restrict__ Bre, const u16* __restrict__ Bim, const u16* __restrict__ Cre,
                                                      const u16* __restrict__ Cim, const u16* __restrict__ b1, const u16* __restrict__ logdt,
                                                      const u16* __restrict__ dskip, const u16* __restrict__ w1, const u16* __restrict__ w2,
                                                      const u16* __restrict__ b2, u16* __restrict__ out, char* ws) {
  LAS char* const S = (LAS char*)0;
  const int tid = threadIdx.x, lane = tid & 63, w = __builtin_amdgcn_readfirstlane(tid >> 6), wq = w & 3;
  const int b = blockIdx.x;
  STAMP_DECL;
  STAMP(0);
  v4u pf0 = {}, pf1 = {};
  if (w == 7) {
    unsigned len;
    asm volatile("s4_code_begin:\n\ts_mov_b32 %0, s4_code_end-s4_code_begin-256" : "=s"(len));
    const v4u sc = mk_srd((const void*)__builtin_amdgcn_s_getpc(), __builtin_amdgcn_readfirstlane(len));
    asm volatile("s_nop 4\n\tbuffer_load_dwordx4 %0, %2, %3, 0 offen\n\tbuffer_load_dwordx4 %1, %4, %3, 0 offen"
                 : "=&v"(pf0), "=&v"(pf1) : "v"((b >> 3) * 1024 + lane * 16), "s"(sc), "v"((b >> 3) * 1024 + lane * 16 + 32768) : "memory");
    *(LAS unsigned*)(S + CNT + 4 * lane) = 0u;
  }
  unsigned tl0 = 0, tl1 = 0;
  asm volatile("" ::"s"(x), "s"(w1), "s"(ws), "s"(Are), "s"(Aim), "s"(Bre), "s"(Bim), "s"(Cre), "s"(Cim), "s"(w2), "s"(logdt), "s"(dskip));
  if (w == 7) {
    const int k = (b >> 3) * 8 + (lane & 7);
    if (lane < 8) asm volatile("global_load_dword %0, %1, %2" : "=v"(tl0) : "v"(k * 65536 + 2048), "s"(ws) : "memory");
    if (lane < 8 && k < 40) asm volatile("global_load_dword %0, %1, %2" : "=v"(tl1) : "v"(k * 65536), "s"(out) : "memory");
  }
  bar();
#define IDS()                                                                                                              \
  int bl = b;                                                                                                              \
  asm volatile("" : "+s"(bl));                                                                                             \
  bl = __builtin_amdgcn_readfirstlane(bl);                                                                                 \
  const int xcd = bl & 7, bi = bl >> 3;                                                                                    \
  const int rp = 4 * (xcd >> 1) + (bi >> 3), cb = 7 * (xcd & 1) + (bi & 7);                                 \
  const bool spare = (bi & 7) == 7;                                         \
  const int c0 = cb * 128 > F - 128 ? F - 128 : cb * 128, gi = rp * 14 + cb, sp = xcd * 4 + (bi >> 3);                      \
  const int yi = spare ? 32 * (sp >> 2) + 28 + (sp & 3) : 32 * (gi / 28) + gi % 28;                                        \
  const int ch00 = spare ? 80 * (sp >> 2) + 56 + 6 * (sp & 3) : 80 * (gi / 28) + 2 * (gi % 28), nbat = spare ? 3 : 1;      \
  const int rt = 8 * (xcd >> 1) + (bi >> 2), ct = 4 * (xcd & 1) + (bi & 3);                                 \
  const int kar = spare ? KARR : KARR + 4 * KCH;                                                                             \
  const int s0 = (cb * 3 + rp * 5 + (rp >> 1)) % 10;             \
  const unsigned w2s = WS_W2T + (ct * 270 + w) * 1024;                                                                      \
  const int fhk = FL_H + (rt >> 1) * 14 + (lane < 14 ? lane : 0);                                                          \
  (void)c0; (void)yi; (void)ch00; (void)nbat; (void)kar; (void)s0; (void)w2s; (void)fhk
  const __amdgpu_buffer_rsrc_t rws = __builtin_amdgcn_make_buffer_rsrc(ws, 0, WS_END, 0x00020000);
  const int h = lane >> 5, g = lane >> 4, q = (lane & 15) >> 2, p = lane & 3, blk = g & 1;
  auto fw_flag = [&](int ct) { const int k = (lane >> 1) * 8 + w; return FL_W + (k < 54 ? k : w) * 10 + ((ct * 80 + (lane & 1) * 79) >> 6); };
  const unsigned k6 = w < 6 ? 49152u : 0u;
  v4u wf2[35];
  const unsigned w2v = lane * 16, w2v6 = w2v + (w < 6 ? 0u : OOB);
  auto load_w2_ = [&](unsigned w2s, int i0, int i1) {
#pragma unroll
    for (int i = 0; i < 35; ++i)
      if (i >= i0 && i < i1) wf2[i] = ld16(rws, i >= 30 ? w2v6 : w2v, w2s + (i % 5) * (54 * 1024) + (i / 5) * 8192);
  };
  auto warm_w2 = [&](int c2, u64 tag) {
    wait_flags(ws, fw_flag(c2), tag);
    load_w2_(WS_W2T + (c2 * 270 + w) * 1024, 0, 35);
#pragma unroll
    for (int i = 0; i < 35; ++i) GOT(wf2[i]);
  };
  const int tt = tid & 255;
  v2u gr[8];
  v4u un[2];
  auto load_stats = [&]() {
#pragma unroll
    for (int i = 0; i < 8; ++i) gr[i] = __builtin_amdgcn_raw_buffer_load_b64(rws, tt * 8, WS_ST + i * 2048, 16);
  };
  auto check_stats = [&](u64 tag) {
    unsigned bad = 0;
#pragma unroll
    for (int i = 0; i < 8; ++i) bad |= gr[i].y ^ (unsigned)tag;
    if (!__all(bad == 0)) {
#pragma unroll
      for (int i = 0; i < 8; ++i) gr[i].x = (unsigned)wait_granule(ws, WS_ST + i * 2048 + tt * 8, tag);
    }
  };
  auto load_u = [&](int ch) {
#pragma unroll
    for (int j = 0; j < 2; ++j) un[j] = ld16(rws, tt * 16, WS_XT + (ch + j) * 4096);
  };
  auto prep = [&](int bt) {
    float rr[8];
#pragma unroll
    for (int e = 0; e < 8; ++e) rr[e] = __builtin_amdgcn_rcpf(bfhi(gr[e].x));
    asm volatile("s_nop 0" : "+v"(rr[0]), "+v"(rr[1]), "+v"(rr[2]), "+v"(rr[3]), "+v"(rr[4]), "+v"(rr[5]), "+v"(rr[6]), "+v"(rr[7]));
#pragma unroll
    for (int e = 0; e < 4; ++e) {
      const float n0 = bflo(gr[2 * e].x), n1 = bflo(gr[2 * e + 1].x);
#pragma unroll
      for (int j = 0; j < 2; ++j) un[j][e] = pk(smul(ssub(bflo(un[j][e]), n0), rr[2 * e]), smul(ssub(bfhi(un[j][e]), n1), rr[2 * e + 1]));
    }
    LAS char* U = S + UCP + (bt & 1) * (2 * UCH) + 32 + tt * 16;
    if (bt >= 2) lds_wait_ge(S, CNT + 16 * 3 + 4 * p, bt - 1);
    if (tt < 16) *(LAS v4u*)(U - 32 - tt * 16 + (tt >> 1) * UST + (tt & 1) * 16) = v4u{0u, 0u, 0u, 0u};
    *(LAS v4u*)U = un[0];
    *(LAS v4u*)(U + UCH) = un[1];
    lds_put(CNT + 16 * 7 + 4 * wq, bt + 1);
    lds_wait_ge(S, CNT + 16 * 7 + 4 * p, bt + 1);
#pragma unroll
    for (int j = 0; j < 2; ++j) {
      const v4u d = un[j]; const v2u pv = *(LAS v2u*)(U + j * UCH - 8);
      const unsigned a0 = __builtin_amdgcn_alignbit(pv.y, pv.x, 16), a1 = __builtin_amdgcn_alignbit(d.x, pv.y, 16), a2 = __builtin_amdgcn_alignbit(d.y, d.x, 16),
                     a3 = __builtin_amdgcn_alignbit(d.z, d.y, 16), a4 = __builtin_amdgcn_alignbit(d.w, d.z, 16);
      *(LAS v4u*)(U + j * UCH + UST) = v4u{a1, a2, a3, a4};
      *(LAS v4u*)(U + j * UCH + 2 * UST) = v4u{pv.y, d.x, d.y, d.z};
      *(LAS v4u*)(U + j * UCH + 3 * UST) = v4u{a0, a1, a2, a3};
    }
    lds_put(CNT + 16 * 8 + 4 * wq, bt + 1);
  };
  u64 tag, fh = 0;
  if (w >= 4) {
    IDS();
    int sn = s0;
    const int u0 = spare ? 448 + 3 * sp : 2 * gi;
    const v4u sx = mk_srd(x, L * D * 2), sw1 = mk_srd(w1, D * F * 2);
    unsigned vx[4], vw[4];
    auto ring_setup = [&]() {
      int ln = lane;
      asm volatile("" : "+v"(ln));
#pragma unroll
      for (int j = 0; j < 4; ++j) {
        const int rx = 8 * j + (ln >> 3);
        vx[j] = (rp * 128 + rx) * (D * 2) + 16 * ((ln & 7) ^ ((rx >> 1) & 7)) - 1024 * j;
        vw[j] = (4 * j + (ln >> 4)) * (F * 2) + c0 * 2 + 16 * ((ln & 15) ^ (((ln >> 4) << 2) | j)) - 1024 * j;
      }
    };
    auto quarter = [&](int st, int slot, int k) {
      const unsigned so = st * 128 + k * (32 * D * 2), sv = st * (64 * F * 2) + k * (16 * F * 2);
      SET_M0(slot * SLOT + k * 4096);
      DMA(sx, vx[0], so, 0); DMA(sx, vx[1], so, 1024); DMA(sx, vx[2], so, 2048); DMA(sx, vx[3], so, 3072);
      SET_M0(slot * SLOT + 16384 + k * 4096);
      DMA(sw1, vw[0], sv, 0); DMA(sw1, vw[1], sv, 1024); DMA(sw1, vw[2], sv, 2048); DMA(sw1, vw[3], sv, 3072);
    };
    if (w == 7) {
      const __amdgpu_buffer_rsrc_t rx = __builtin_amdgcn_make_buffer_rsrc((void*)x, 0, L * D * 2, 0x00020000),
                                   rw2 = __builtin_amdgcn_make_buffer_rsrc((void*)w2, 0, D * F * 2, 0x00020000);
      const u64 ep = ld_word(ws, 0);
      v4u xv[10], wv[3][4];
#pragma unroll
      for (int i = 0; i < 10; ++i) xv[i] = __builtin_amdgcn_raw_buffer_load_b128(rx, (lane >> 3) * (D * 2) + (lane & 7) * 16 + i * 128, b * (8 * D * 2), 0);
#pragma unroll
      for (int k = 0; k < 3; ++k) {
        const int u = u0 + k;
        const unsigned vo = (lane >> 3) * (D * 2) + (lane & 7) * 16 + ((k < 2 || spare) && u < 540 ? 0u : OOB);
#pragma unroll
        for (int i = 0; i < 4; ++i) wv[k][i] = __builtin_amdgcn_raw_buffer_load_b128(rw2, vo, ((u / 10) * 32 + 8 * i) * (D * 2) + (u % 10) * 128, 0);
      }
      PIN();
      ring_setup();
      const u64 tg = next_tag(ep);
      lds_put(TAGL, (unsigned)tg); lds_put(TAGL + 4, (unsigned)(tg >> 32));
      lds_set(S, TAGL + 8, 1);
      GOT(pf0); GOT(pf1); GOT(tl0); GOT(tl1);
      float sm[2] = {}, sq[2] = {};
#pragma unroll
      for (int i = 0; i < 10; ++i) { sm[i & 1] = sum8(xv[i], sm[i & 1]); sq[i & 1] = dot8(xv[i], xv[i], sq[i & 1]); }
      float sum = sm[0] + sm[1], ssq = sq[0] + sq[1];
#pragma unroll
      for (int m = 1; m < 8; m <<= 1) { sum += __shfl_xor(sum, m); ssq += __shfl_xor(ssq, m); }
      const float mean = sum * (1.f / D), var = fmaxf(ssq * (1.f / D) - mean * mean, 0.f);
      const unsigned ms = pk(mean, __builtin_amdgcn_sqrtf(rbf(rbf(var) + 1e-5f)));
      if ((lane & 7) == 0) __hip_atomic_store((GAS u64*)(ws + WS_ST + (lane >> 3) * 2048 + b * 8), (tg << 32) | ms, __ATOMIC_RELAXED, __HIP_MEMORY_SCOPE_AGENT);
      STAMP(1);
      LAS char* U = S + (spare ? W2U_S : W2U_G);
      LAS char* B = U + (4 * (blk + 4 * (g >> 1)) + q) * 128 + 8 * p;
      GOT(wv[2][0]); GOT(wv[2][1]); GOT(wv[2][2]); GOT(wv[2][3]);
#pragma unroll
      for (int k = 0; k < 3; ++k) {
        const int u = u0 + k;
        if ((k < 2 || spare) && u < 540) {
#pragma unroll
          for (int i = 0; i < 4; ++i) *(LAS v4u*)(U + i * 1024 + lane * 16) = wv[k][i];
#pragma unroll
          for (int n = 0; n < 4; ++n) st16(rws, trd2(B + 32 * n, B + 32 * n + 1024), lane * 16 + WS_W2T + (((u % 10) * 4 + n) * 54 + u / 10) * 1024);
        }
      }
    } else if (!spare) {
      ring_setup();
#pragma unroll 1
      for (int t = wq; t < 12; t += 3) {
        if (t >= 9) { WAITV(8); lds_put(CNT + 28, 1); lds_put(CNT + 16 + 4 * wq, 1); }
        quarter(sn + (t >> 2) >= 10 ? sn + (t >> 2) - 10 : sn + (t >> 2), t >> 2, t & 3);
      }
      WAITV(8);
      STAMP(1);
    } else if (sp < 27) {
      const __amdgpu_buffer_rsrc_t rw1 = __builtin_amdgcn_make_buffer_rsrc((void*)w1, 0, D * F * 2, 0x00020000);
      v4u v[27];
#pragma unroll
      for (int i = 0; i < 27; ++i) v[i] = __builtin_amdgcn_raw_buffer_load_b128(rw1, (216 * wq + 8 * i + (lane >> 3)) * (F * 2) + (lane & 7) * 16, sp * 128, 0);
      PIN();
      f2 a[4] = {};
#pragma unroll
      for (int i = 0; i < 27; ++i)
#pragma unroll
        for (int e = 0; e < 4; ++e) a[e] += unpk(v[i][e]);
      *(LAS f4*)(S + (wq * 64 + lane) * 32) = f4{a[0].x, a[0].y, a[1].x, a[1].y};
      *(LAS f4*)(S + (wq * 64 + lane) * 32 + 16) = f4{a[2].x, a[2].y, a[3].x, a[3].y};
      lds_put(CNT + 16 + 12, 1);
      if (lds_arrive(S, 1, wq)) {
        float t = 0.f;
#pragma unroll
        for (int i = 0; i < 24; ++i) t += *(LAS float*)(S + i * 256 + lane * 4);
        __hip_atomic_store((GAS u64*)(ws + WS_CS + (sp * 64 + lane) * 8), (get_tag(S) << 32) | __float_as_uint(t), __ATOMIC_RELAXED, __HIP_MEMORY_SCOPE_AGENT);
      }
    }
    int fwk = fw_flag(ct);
    GOT(fwk);
    STAMP(2);
    u64 pw = 0, px = 0;
    const int fxk = FL_X + (lane & 15) * 10 + ((ch00 + ((lane & 16) ? 2 * nbat - 1 : 0)) >> 6);
    if (!spare) {
      bar();
      STAMP(6);
      tag = rd_tag(S);
      sn = sn >= 7 ? sn - 7 : sn + 3;
#pragma unroll 1
      for (int n = 0; n < 6; ++n) {
        quarter(sn, (n + 3) & 3, wq);
        sn = sn == 9 ? 0 : sn + 1;
        WAITV(8);
        if (n == 0 && w == 7) { if (lane < 2) put_flag(ws, FL_W + u0 + lane, tag); }
        bar_raw();
      }
      quarter(sn, 1, wq);
      pw = ld_hidden(ws, WS_FLAG + 128u * fwk); px = ld_hidden(ws, WS_FLAG + 128u * fxk);
      load_stats();
      WAITV(18);
      bar_raw();
      STAMP(4);
      WAITV(8);
      GOT(pw); GOT(px);
      check_flags(ws, fxk, px, tag);
      load_u(ch00);
      ORDER();
      bar_raw();
      STAMP(5);
      check_flags(ws, fwk, pw, tag);
    } else {
      tag = get_tag(S);
      if (w == 7) {
        WAITV(0);
        if (lane < 3 && u0 + lane < 540) put_flag(ws, FL_W + u0 + lane, tag);
      }
      if (WARM && (bi >> 3) != 3) warm_w2(4 * (xcd & 1) + (bi >> 3), tag);
      load_stats();
      wait_flags(ws, fwk, tag);
    }
    load_w2_(w2s, 0, NB1);
    PIN();
    if (spare) {
      wait_flags(ws, fxk, tag);
      load_u(ch00);
      GOT(un[0]); GOT(un[1]);
      bar_raw();
    }
    STAMP(1);
    check_stats(tag);
    prep(0);
    STAMP(12);
    LAS char* Kp = S + kar + 512 + 32 * ((lane & 15) - (g >> 1)) + 16 * blk - 64 * wq - 256;
    LAS char* const Up0 = S + UCP + q * UST + 32 + 32 * (g >> 1) - 16 * blk + 8 * p + 64 * wq - 8;
    LAS char* const Pp0 = S + wq * 8192 + g * 256 + (((lane & 15) ^ (2 * g)) * 16);
#pragma unroll 1
    for (int cj = 0; cj < 2 * nbat; ++cj) {
      const int bt = cj >> 1;
      if (!(cj & 1)) lds_wait_ge(S, CNT + 16 * 8 + 4 * p, bt + 1);
      LAS char* Up = Up0 + (cj & 3) * UCH;
      if (cj == 2 * nbat - 1) fh = ld_hidden(ws, WS_FLAG + 128u * fhk);
      f4 acc[8];
      v4u kf[8], kg[8], ua[2], ub[2];
      auto rd_u = [&](int s) { ua[s & 1] = trd2(Up + 512 * s + 8, Up + 512 * s); ub[s & 1] = trd2(Up + 512 * (7 - s) + 264, Up + 512 * (7 - s) + 256); kg[s] = *(LAS v4u*)(Kp + 512 * s); };
      rd_u(0);
#pragma unroll
      for (int e = 7; e >= 0; --e) kf[e] = *(LAS v4u*)(Kp + 512 * e + 256);
      if (cj == 0) {
        lds_wait(S, 6);
        STAMP(13);
      }
      PIN();
#pragma unroll
      for (int s = 0; s < 8; ++s) {
        if (s < 7) rd_u(s + 1);
#pragma unroll
        for (int R = 7; R >= s; --R) acc[R] = __builtin_amdgcn_mfma_f32_16x16x32_bf16(asbf(ua[s & 1]), asbf(kf[R - s]), s ? acc[R] : f4{0.f, 0.f, 0.f, 0.f}, 0, 0, 0);
#pragma unroll
        for (int R = 7 - s; R < 8; ++R) acc[R] = __builtin_amdgcn_mfma_f32_16x16x32_bf16(asbf(ub[s & 1]), asbf(kg[R - 7 + s]), acc[R], 0, 0, 0);
#pragma unroll
        for (int i = 0; i < 5; ++i) { __builtin_amdgcn_sched_group_barrier(0x8, 1, 0); __builtin_amdgcn_sched_group_barrier(0x80, 1, 0); }
        PIN();
      }
      if (bt) lds_wait_ge(S, CNT + 16 * 9 + 4 * p, cj - 1);
#pragma unroll
      for (int R = 0; R < 8; ++R) *(LAS f4*)(Pp0 + (cj & 1) * SLOT + R * 1024) = acc[R];
      lds_put(CNT + 16 * (2 + (cj & 1)) + 4 * wq, bt + 1);
      Kp += KCH;
      if (cj == 1 && spare) STAMP(2);
    }
    STAMP(7);
    WAITV(0);
    load_w2_(w2s, NB1, 35);
  } else {
    IDS();
    const int wf = w >> 1, tg = w & 1;
    unsigned xa[4], wa[2][2], pn[6];
    unsigned lgn, dskn;
    auto fetch = [&](int bt) {
      const int ch = ch00 + 2 * bt + wf, o = ch * N + tg * 64 + lane;
      pn[0] = Are[o]; pn[1] = Aim[o]; pn[2] = Bre[o]; pn[3] = Bim[o]; pn[4] = Cre[o]; pn[5] = Cim[o];
      lgn = logdt[ch]; dskn = dskip[ch];
      PIN();
    };
    fetch(0);
    int fwk = fw_flag(ct);
    GOT(fwk);
#pragma unroll
    for (int i = 0; i < 4; ++i) {
      const int row = 64 * tg + (lane & 31), c = 2 * wf + (i >> 1), t2 = i & 1;
      xa[i] = (row * 128 + 16 * (h ^ ((row >> 1) & 7))) ^ (32 * i);
      wa[i >> 1][i & 1] = 16384 + 256 * (8 * h + 4 * t2 + q) + 64 * (c ^ q) + 16 * ((2 * blk + (p >> 1)) ^ (2 * h + t2)) + 8 * (p & 1);
      GOT(xa[i]); GOT(wa[i >> 1][i & 1]);
    }
    PIN();
#pragma unroll 1
    for (int bt = 0; bt < nbat; ++bt) {
      const unsigned p0 = pn[0], p1 = pn[1], p2 = pn[2], p3 = pn[3], p4 = pn[4], p5 = pn[5];
      const float lg = bflo(lgn), dsk = bflo(dskn);
      if (bt + 1 < nbat) fetch(bt + 1);
      if (bt) lds_wait_ge(S, CNT + 16 * 5 + 4 * (w ^ 1), bt);
      LAS unsigned* T = (LAS unsigned*)(S + TAB + w * TW) + lane;
      LAS char* Tr = S + TAB + w * TW + g * 16 + (lane & 15) * TROW;
      constexpr int R = TROW / 4;
      const float dt = rbf(__expf(lg)), a = dt * bflo(p0) * 1.4426950408889634f;
      float th = dt * bflo(p1) * 0.15915494309189535f;
      th -= __builtin_rintf(th);
      const f2 e1 = cexpl(a, th, 1.f), e128 = cexpl(a, th, 128.f), e8 = cexpl(a, -th, 8.f);
      const cm m1 = mkm(e1), m128 = mkm(e128), m8 = mkm(e8), m16 = mkm(cmul(e8, m8));
      const f2 e2 = cmul(e1, m1);
      const cm m2 = mkm(e2), m4 = mkm(cmul(e2, m2));
      f2 y = cmul(f2{bflo(p4), -bflo(p5)}, mkm(f2{dt, dt} * f2{bflo(p2), -bflo(p3)})), y2 = cmul(y, m8), t = cexpl(a, th, 1024.f);
#pragma unroll
      for (int i = 0; i < 8; ++i) {
        T[(2 * i) * R] = pk2(y); T[(2 * i + 1) * R] = pk2(y2); T[(16 + i) * R] = pk2(t);
        if (i < 7) { y = cmul(y, m16); y2 = cmul(y2, m16); }
        t = cmul(t, m128);
      }
      T[24 * R] = pk2(t);
      v4u fy[4], fa[4][2];
      f4 ka[3] = {};
#pragma unroll
      for (int ks = 0; ks < 4; ++ks) { fy[ks] = *(LAS v4u*)(Tr + ks * 64); fa[ks][0] = *(LAS v4u*)(Tr + 16 * TROW + ks * 64); }
      PIN();
      f2 u = {1.f, 0.f}, v = e128, z = cmul(e128, m128);
#pragma unroll
      for (int i = 0; i < 8; ++i) {
        T[i * R] = pk2(u); T[(8 + i) * R] = pk2(v);
        if (i < 7) { u = cmul(u, m1); v = cmul(v, m1); }
        if (i < 2) {
          const f2 z2 = cmul(z, m2);
          T[(16 + 4 * i) * R] = pk2(z); T[(17 + 4 * i) * R] = pk2(z2); T[(18 + 4 * i) * R] = pk2(cmul(z, m4)); T[(19 + 4 * i) * R] = pk2(cmul(z2, m4));
        } else if (i < 6) {
          T[(20 + 2 * i) * R] = pk2(z); T[(21 + 2 * i) * R] = pk2(cmul(z, m4));
        }
        if (i < 5) z = cmul(z, m128);
      }
      PIN();
#pragma unroll
      for (int ks = 0; ks < 4; ++ks) ka[2] = __builtin_amdgcn_mfma_f32_16x16x32_bf16(asbf(fa[ks][0]), asbf(fy[ks]), ka[2], 0, 0, 0);
#pragma unroll
      for (int ks = 0; ks < 4; ++ks) { fa[ks][0] = *(LAS v4u*)(Tr + ks * 64); fa[ks][1] = *(LAS v4u*)(Tr + 16 * TROW + ks * 64); }
      PIN();
#pragma unroll
      for (int ks = 0; ks < 4; ++ks)
#pragma unroll
        for (int i = 0; i < 2; ++i) ka[i] = __builtin_amdgcn_mfma_f32_16x16x32_bf16(asbf(fa[ks][i]), asbf(fy[ks]), ka[i], 0, 0, 0);
      if (bt == 0) STAMP(1);
      {
        LAS char* X = S + TAB + w * TW + 4608 + lane * 16;
        *(LAS f4*)X = tg ? ka[0] : ka[1];
        *(LAS f4*)(X + 1024) = ka[2];
        lds_put(CNT + 16 * 4 + 4 * w, bt + 1);
        lds_wait_ge(S, CNT + 16 * 4 + 4 * (w ^ 1), bt + 1);
        LAS char* Y = S + TAB + (w ^ 1) * TW + 4608 + lane * 16;
        f4 km = (tg ? ka[1] : ka[0]) + *(LAS f4*)Y, k2 = ka[2] + *(LAS f4*)(Y + 1024);
        GOT(km); GOT(k2);
        lds_put(CNT + 16 * 5 + 4 * w, bt + 1);
        const int c = lane & 15;
        LAS char* K = S + kar + (2 * bt + wf) * KCH;
        LAS u16* T1 = (LAS u16*)(S + TAB + w * TW);
        const unsigned q01 = pk2(k2.xy), q23 = pk2(k2.zw);
        if (!tg) {
          if (lane == 0) km.x += dsk;
          *(LAS v2u*)(K + 512 + 256 * (g >> 1) + 16 * c + 8 * blk) = v2u{pk2(km.xy), pk2(km.zw)};
          LAS u16* T3 = T1 + 64 * g + c;
          T3[0] = (u16)q01; T3[16] = (u16)(q01 >> 16); T3[32] = (u16)q23; T3[48] = (u16)(q23 >> 16);
          ORDER();
          const unsigned c0_ = T1[lane], c1 = T1[1 + lane], d0 = T1[64 + lane], d1 = T1[65 + lane];
          if (h) *(LAS v4u*)(K + 16 * (lane & 31)) = v4u{0u, 0u, 0u, 0u};
          *(LAS v4u*)(K + 2560 + 16 * lane) = v4u{c0_ * 0x10001u, c0_ * 0x10001u, ((lane & 1) ? c1 : c0_) | (c1 << 16), c1 * 0x10001u};
          *(LAS v4u*)(K + 3584 + 16 * lane) = v4u{d0 * 0x10001u, d0 * 0x10001u, ((lane & 1) ? d1 : d0) | (d1 << 16), d1 * 0x10001u};
        } else {
          const unsigned p01 = pk2(km.xy), p23 = pk2(km.zw);
          if (g < 2) *(LAS v2u*)(T1 + 64 * g + 4 * c) = v2u{p01, p23};
          else { *(LAS unsigned*)(T1 + 64 * g + 2 * c) = p01; *(LAS unsigned*)(T1 + 64 * g + 32 + 2 * c) = p23; }
          if (lane == 0) T1[256] = (u16)q01;
          ORDER();
          const v2u a1 = *(LAS v2u*)(T1 + 4 * (lane & 31));
          const unsigned a4 = T1[4 * (lane & 31) + 4], b0 = *(LAS unsigned*)(T1 + 128 + 2 * lane), b2_ = T1[130 + 2 * lane];
          if (!h) *(LAS v4u*)(K + 1024 + 16 * lane) = v4u{(a1.x & 0xffffu) * 0x10001u, (a1.x >> 16) | (a1.y << 16), (a1.y & 0xffffu) * 0x10001u, (a1.y >> 16) | (a4 << 16)};
          *(LAS v4u*)(K + 1536 + 16 * lane) = v4u{(b0 & 0xffffu) * 0x10001u, b0, (b0 >> 16) * 0x10001u, b2_ * 0x10001u};
        }
      }
    }
    if (cb < 10 && !spare) {
      lds_wait(S, 1);
      v2u xt[8];
#pragma unroll
      for (int m = 0; m < 8; ++m) {
        const int rr = 32 * g + 4 * m + q;
        xt[m] = trd(S + 128 * rr + 16 * ((2 * w + (p >> 1)) ^ ((rr >> 1) & 7)) + 8 * (p & 1));
      }
      LAS char* X = S + TAB + w * TW;
#pragma unroll
      for (int k = 0; k < 4; ++k) *(LAS v4u*)(X + (lane & 15) * 272 + g * 64 + k * 16) = v4u{xt[2 * k].x, xt[2 * k].y, xt[2 * k + 1].x, xt[2 * k + 1].y};
#pragma unroll
      for (int k = 0; k < 4; ++k)
        st16(rws, *(LAS v4u*)(X + (4 * k + g) * 272 + (lane & 15) * 16), WS_XT + (s0 * 64 + 16 * w + 4 * k + g) * 4096 + rp * 256 + (lane & 15) * 16);
    }
    auto publish = [&](int j) {
      lds_wait_ge(S, CNT + 16 * (2 + (j & 1)) + 4 * p, (j >> 1) + 1);
      const unsigned pa = (j & 1) * SLOT + (tid >> 5) * 1024 + (((tid >> 1) & 15) ^ (4 * (tid & 1))) * 16 + (tid & 1) * 512;
      f4 lo[4], hi[4], l, m;
#pragma unroll
      for (int v = 0; v < 4; ++v) { lo[v] = *(LAS f4*)(S + pa + v * 8192); hi[v] = *(LAS f4*)(S + (pa ^ 32u) + 256 + v * 8192); }
#pragma unroll
      for (int e = 0; e < 4; ++e) { l[e] = sadd(sadd(lo[0][e], lo[1][e]), sadd(lo[2][e], lo[3][e])); m[e] = sadd(sadd(hi[0][e], hi[1][e]), sadd(hi[2][e], hi[3][e])); }
      GOT(l); GOT(m);
      lds_put(CNT + 16 * 9 + 4 * wq, j + 1);
      st16(rws, v4u{pk2(l.xy), pk2(l.zw), pk2(m.xy), pk2(m.zw)}, tid * 16 + WS_YT + (ch00 + j) * 4096);
    };
    STAMP(2);
    if (!spare) {
      f16v acc[2][2] = {};
      v4u fx[4][2], fw[4][2];
      auto rd = [&](int set, int so, int ks) {
        fw[set][0] = trd2(S + wa[0][0] + so + 4096 * ks, S + wa[0][1] + so + 4096 * ks);
        fx[set][0] = *(LAS v4u*)(S + xa[ks] + so);
        fx[set][1] = *(LAS v4u*)(S + xa[ks] + so + 4096);
        fw[set][1] = trd2(S + wa[1][0] + so + 4096 * ks, S + wa[1][1] + so + 4096 * ks);
      };
      auto mm = [&](int set) {
#pragma unroll
        for (int i = 0; i < 4; ++i) acc[i >> 1][i & 1] = __builtin_amdgcn_mfma_f32_32x32x16_bf16(asbf(fw[set][i >> 1]), asbf(fx[set][i & 1]), acc[i >> 1][i & 1], 0, 0, 0);
#pragma unroll
        for (int i = 0; i < 3; ++i) { __builtin_amdgcn_sched_group_barrier(0x8, 1, 0); __builtin_amdgcn_sched_group_barrier(0x100, 2, 0); }
        __builtin_amdgcn_sched_group_barrier(0x8, 1, 0);
        PIN();
      };
      u64 fw_seen = 0, g0 = 0, g1 = 0, gc = 0;
      unsigned b1v = 0;
      const unsigned o0 = WS_ST + (lane & 7) * 2048 + (rp * 16 + 8 * tg + ((lane & 31) >> 3)) * 8, oc = WS_CS + (c0 + 64 * wf + lane) * 8;
      asm volatile("" : "+v"(acc[0][0]), "+v"(acc[0][1]), "+v"(acc[1][0]), "+v"(acc[1][1]));
      bar();
      STAMP(6);
      tag = rd_tag(S);
      rd(0, 0, 0); rd(1, 0, 1);
      PIN();
#pragma unroll 1
      for (int n = 0; n < 5; ++n) {
        if (n == 4) { fw_seen = ld_hidden(ws, WS_FLAG + 128u * fwk); g0 = ld_hidden(ws, o0); g1 = ld_hidden(ws, o0 + 32); gc = ld_hidden(ws, oc);
            asm volatile("global_load_ushort %0, %1, %2" : "=v"(b1v) : "v"((c0 + 64 * wf + lane) * 2), "s"(b1) : "memory"); }
        rd(2, 0, 2); mm(0);
        rd(3, 0, 3); mm(1);
        rd(0, SLOT, 0); mm(2);
        rd(1, SLOT, 1); mm(3);
        if (n == 4) {
          WAITV(0); GOT(fw_seen); GOT(g0); GOT(g1); GOT(gc); GOT(b1v);
          if (!tag_ok(gc, tag)) gc = wait_granule(ws, oc, tag);
          *(LAS unsigned*)(S + COLT + w * 512 + 256 + lane * 4) = b1v << 16;
          *(LAS unsigned*)(S + COLT + w * 512 + lane * 4) = (unsigned)gc;
          if (!tag_ok(g0, tag)) g0 = wait_granule(ws, o0, tag);
          if (!tag_ok(g1, tag)) g1 = wait_granule(ws, o0 + 32, tag);
          PIN();
        } else {
          if (n == 1 && w == 1 && cb < 10 && lane == 0) put_flag(ws, FL_X + rp * 10 + s0, tag);
          bar_raw();
        }
        rd(2, SLOT, 2); mm(0);
        rd(3, SLOT, 3); mm(1);
#pragma unroll
        for (int i = 0; i < 4; ++i) { xa[i] ^= 2 * SLOT; wa[i >> 1][i & 1] ^= 2 * SLOT; }
        rd(0, 0, 0); mm(2);
        rd(1, 0, 1); mm(3);
        if (n == 0) WAITV(0);
        if (n < 4) bar_raw();
      }
      STAMP(3);
      const unsigned hv = WS_HT + (((rp * 8 + 4 * tg + blk) * 54 + (c0 >> 5) + 2 * wf) * 64 + (lane & 15) + 16 * h) * 16 + ((cb == 13 && wf == 0) ? OOB : 0u);
#pragma unroll
      for (int c = 0; c < 2; ++c) {
        f4 cs[4], bb[4];
#pragma unroll
        for (int a = 0; a < 4; ++a) { LAS char* ct_ = S + COLT + w * 512 + (32 * c + 8 * a + 4 * h) * 4; cs[a] = *(LAS f4*)ct_; bb[a] = *(LAS f4*)(ct_ + 256); }
#pragma unroll
        for (int tt = 0; tt < 2; ++tt) {
          const float rs = __builtin_amdgcn_rcpf(bfhi((unsigned)(tt ? g1 : g0))), mr = -bflo((unsigned)(tt ? g1 : g0)) * rs;
          unsigned o[8];
#pragma unroll
          for (int a = 0; a < 4; ++a) {
#pragma unroll
            for (int e = 0; e < 2; ++e) {
              const f2 v = fma2(f2{acc[c][tt][4 * a + 2 * e], acc[c][tt][4 * a + 2 * e + 1]}, f2{rs, rs}, fma2(f2{mr, mr}, e ? cs[a].zw : cs[a].xy, e ? bb[a].zw : bb[a].xy));
              const f2 z = v * fma2(v * v, f2{-0.10294324f, -0.10294324f}, f2{-2.30220819f, -2.30220819f});
              const f2 dn = f2{__builtin_amdgcn_exp2f(z.x), __builtin_amdgcn_exp2f(z.y)} + f2{1.f, 1.f};
              o[2 * a + e] = pk2(v * f2{__builtin_amdgcn_rcpf(dn.x), __builtin_amdgcn_rcpf(dn.y)});
            }
          }
          st16(rws, v4u{o[0], o[1], o[2], o[3]}, hv + (tt * 108 + c) * 1024);
          st16(rws, v4u{o[4], o[5], o[6], o[7]}, hv + (tt * 108 + c) * 1024 + 512);
          PIN();
        }
      }
      STAMP(4);
      lds_put(CNT + 16 * 6 + 4 * wq, 1);
      check_flags(ws, fwk, fw_seen, tag);
      load_w2_(w2s, 0, 8);
      WAITV(8);
      if (lds_arrive(S, 0, wq) && lane == 0) put_flag(ws, FL_H + gi, tag);
      STAMP(5);
    } else {
      lds_put(CNT + 16 * 6 + 4 * wq, 1);
      load_stats();
      bar();
      tag = rd_tag(S);
      check_stats(tag);
#pragma unroll 1
      for (int bt = 1; bt < 3; ++bt) {
        load_u(ch00 + 2 * bt);
        if (bt == 1) lds_wait_ge(S, CNT + 16 * 8 + 4 * p, 1);
        prep(bt);
        publish(2 * bt - 2);
        publish(2 * bt - 1);
      }
      wait_flags(ws, fwk, tag);
      load_w2_(w2s, 0, 8);
    }
    load_w2_(w2s, 8, 35);
    publish(2 * nbat - 2);
    fh = ld_hidden(ws, WS_FLAG + 128u * fhk);
    publish(2 * nbat - 1);
    STAMP(7);
    WAITV(1);
  }
  {
    IDS();
    GOT(fh);
    check_flags(ws, fhk, fh, tag);
    STAMP(8);
    v4u hf[7];
    unsigned hb[4];
#pragma unroll
    for (int pt = 0; pt < 4; ++pt) hb[pt] = WS_HT + ((rt * 4 + ((pt + ct) & 3)) * 54 + w) * 1024;
#pragma unroll
    for (int i = 0; i < 7; ++i) hf[i] = ld16(rws, w2v, hb[0] + (i < 6 ? i * 8192u : k6));
    PIN();
    if (w < 4 && spare) {
      WAITV(7);
      if (lds_arrive(S, 10, wq) && lane == 0) put_flag(ws, FL_Y + yi, tag);
    }
    bar();
    STAMP(9);
    LAS char* Pw = S + w * GW + g * 256 + (((lane & 15) + g) & 15) * 16;
    const __amdgpu_buffer_rsrc_t rx = __builtin_amdgcn_make_buffer_rsrc((void*)x, 0, L * D * 2, 0x00020000),
                                 ro = __builtin_amdgcn_make_buffer_rsrc(out, 0, L * D * 2, 0x00020000),
                                 rb2 = __builtin_amdgcn_make_buffer_rsrc((void*)b2, 0, D * 2, 0x00020000);
    const int fyk = FL_Y + ct * 32 + (lane & 31);
    unsigned tpr[3], tov[3], tyr[3];
    v2u xr[3], bb[3], yb0;
    v4u yv[2];
    auto task_setup = [&](int i) {
      const int id = i == 0 ? w : i == 1 ? w + 8 : w + 15, pt = id / 5, idx = (id % 5) * 64 + lane, et = idx / 20, eq = idx % 20, t = ((pt + ct) & 3) * 16 + et;
      const bool on = i == 0 || (i == 1 ? w < 7 : w < 5);
      tpr[i] = (pt >= 3 ? 0 : pt) * GBUF + (eq >> 2) * GT + (eq & 3) * 256 + ((et + (eq & 3)) & 15) * 16;
      tov[i] = ((rt * 64 + t) * D + ct * 80 + 4 * eq) * 2 + (on ? 0u : OOB);
      tyr[i] = YT2 + t * YT2ROW + 8 * eq;
      xr[i] = __builtin_amdgcn_raw_buffer_load_b64(rx, tov[i], 0, 0);
      bb[i] = __builtin_amdgcn_raw_buffer_load_b64(rb2, (ct * 80 + 4 * eq) * 2, 0, 0);
    };
    auto reduce = [&](int i) {
      f4 pr[8];
#pragma unroll
      for (int v = 0; v < 8; ++v) pr[v] = *(LAS f4*)(S + tpr[i] + v * GW);
      const f4 sa = ((pr[0] + pr[1]) + (pr[2] + pr[3])) + ((pr[4] + pr[5]) + (pr[6] + pr[7]));
      return v2u{pk2(unpk(pk2(sa.xy)) + unpk(bb[i].x)), pk2(unpk(pk2(sa.zw)) + unpk(bb[i].y))};
    };
    auto finish = [&](int i, v2u yb) {
      const v2u y = *(LAS v2u*)(S + tyr[i]);
      __builtin_amdgcn_raw_buffer_store_b64(v2u{pk2(unpk(pk2(unpk(xr[i].x) + unpk(y.x))) + unpk(yb.x)), pk2(unpk(pk2(unpk(xr[i].y) + unpk(y.y))) + unpk(yb.y))}, ro, tov[i], 0, 0);
    };
    f4 acc[2][5];
#pragma unroll
    for (int pt = 0; pt < 4; ++pt) {
#pragma unroll
      for (int j = 0; j < 7; ++j) {
        if (pt == 3 && j >= 5) {
          const int c = lane + 64 * (j - 5);
          LAS u16* d = (LAS u16*)(S + YT2 + (c & 7) * 8 * YT2ROW) + 10 * w + (c >> 3);
          if (c < 80) {
#pragma unroll
            for (int e = 0; e < 4; ++e) { d[(2 * e) * (YT2ROW / 2)] = (u16)yv[j - 5][e]; d[(2 * e + 1) * (YT2ROW / 2)] = (u16)(yv[j - 5][e] >> 16); }
          }
        }
        if (j >= 1 && j < 6 && pt >= 1) *(LAS f4*)(Pw + (pt - 1) * GBUF + (j - 1) * GT) = acc[(pt - 1) & 1][j - 1];
#pragma unroll
        for (int n = 0; n < 5; ++n)
          acc[pt & 1][n] = __builtin_amdgcn_mfma_f32_16x16x32_bf16(asbf(wf2[5 * j + n]), asbf(hf[j]), j ? acc[pt & 1][n] : f4{0.f, 0.f, 0.f, 0.f}, 0, 0, 0);
        if (pt < 3) hf[j] = ld16(rws, w2v, hb[pt + 1] + (j < 6 ? j * 8192u : k6));
        PIN();
        if (pt == 0 && j == 2 && w < 4 && !spare) {
          WAITV(7);
          if (lds_arrive(S, 10, wq) && lane == 0) put_flag(ws, FL_Y + yi, tag);
        }
        if (pt == 1 && j == 3) fh = ld_hidden(ws, WS_FLAG + 128u * fyk);
        if (pt == 2 && j == 3) {
          WAITV(7);
          GOT(fh);
          check_flags(ws, fyk, fh, tag);
#pragma unroll
          for (int k = 0; k < 2; ++k) {
            const int c = lane + 64 * k;
            yv[k] = ld16(rws, (10 * w + (c >> 3)) * 4096 + (c & 7) * 16 + (c < 80 ? 0u : OOB), WS_YT + ct * 80 * 4096 + rt * 128);
          }
          PIN();
        }
      }
      if (pt == 0) STAMP(11);
      if (pt == 1) STAMP(14);
      if (pt == 2) { task_setup(0); task_setup(1); task_setup(2); PIN(); }
    }
    if (w < 4) STAMP(12);
    bar();
    yb0 = reduce(0);
    GOT(yb0);
    bar();
#pragma unroll
    for (int n = 0; n < 5; ++n) *(LAS f4*)(Pw + n * GT) = acc[1][n];
    finish(0, yb0);
    finish(1, reduce(1));
    bar();
    if (w < 4) STAMP(13);
    finish(2, reduce(2));
  }
  STAMP(10);
  STAMP_OUT();
  if (b == 0 && w == 7) {
#pragma unroll 1
    for (unsigned s = 0, j = 0; s < SPIN_CAP && j < 4; ++s)
      if (__all((unsigned)(ld_word(ws, WS_ST + (64 * j + lane) * 8) >> 32) == (unsigned)tag)) ++j;
    if (lane == 0) __hip_atomic_store((GAS u64*)ws, tag, __ATOMIC_RELAXED, __HIP_MEMORY_SCOPE_AGENT);
  }
  asm volatile("s4_code_end:");
}
}

extern "C" void kernel_launch(void* const* d_in, const int* in_sizes, int n_in, void* d_out, int out_size, void* d_ws, size_t ws_size,
                              hipStream_t stream) {
  (void)in_sizes; (void)n_in; (void)out_size; (void)ws_size;
  (void)hipFuncSetAttribute((const void*)s4mlp_kernel, hipFuncAttributeMaxDynamicSharedMemorySize, LDS_BYTES);
  s4mlp_kernel<<<256, 512, LDS_BYTES, stream>>>((const u16*)d_in[0], (const u16*)d_in[1], (const u16*)d_in[2], (const u16*)d_in[3], (const u16*)d_in[4],
                                                (const u16*)d_in[5], (const u16*)d_in[6], (const u16*)d_in[11], (const u16*)d_in[7], (const u16*)d_in[8],
                                                (const u16*)d_in[10], (const u16*)d_in[12], (const u16*)d_in[13], (u16*)d_out, (char*)d_ws);
}
```

```cpp
#include <hip/hip_runtime.h>
#include <hip/hip_bf16.h>
#include <stdint.h>

namespace {
typedef unsigned short u16;
typedef unsigned long long u64;
typedef __bf16 bf8 __attribute__((ext_vector_type(8)));
typedef __bf16 bf2 __attribute__((ext_vector_type(2)));
typedef short s4 __attribute__((ext_vector_type(4)));
typedef float f16v __attribute__((ext_vector_type(16)));
typedef float f4 __attribute__((ext_vector_type(4)));
typedef float f2 __attribute__((ext_vector_type(2)));
typedef unsigned v4u __attribute__((ext_vector_type(4)));
typedef unsigned v2u __attribute__((ext_vector_type(2)));
#define LAS __attribute__((address_space(3)))
#define GAS __attribute__((address_space(1)))
#define DI __device__ __forceinline__

constexpr int L = 2048, D = 640, N = 128, F = 1728;
constexpr unsigned WS_FLAG = 4096;
constexpr int FL_H = 0, FL_X = 224, FL_Y = 384, FL_W = 640;
constexpr unsigned WS_ST = 256u << 10;
constexpr unsigned WS_CS = WS_ST + 2048 * 8;
constexpr unsigned WS_HT = 1u << 20;
constexpr unsigned WS_W2T = WS_HT + 128u * 54 * 1024;
constexpr unsigned WS_XT = WS_W2T + 40u * 54 * 1024;
constexpr unsigned WS_YT = WS_XT + 640u * 4096;
constexpr unsigned WS_END = WS_YT + 640u * 4096;
constexpr unsigned WS_DBG = 32u << 20;
constexpr unsigned OOB = 0x40000000u;
constexpr int LDS_BYTES = 163840;
constexpr int SLOT = 32768;
constexpr int TAB = 98304, TROW = 272, TW = 32 * TROW;
constexpr int CNT = TAB + 4 * TW, TAGL = CNT + 240;
constexpr int COLT = CNT + 256;
constexpr int W2U_G = COLT + 2048, W2U_S = 16384;
constexpr int KCH = 4608, KARR = LDS_BYTES - 6 * KCH;
constexpr int UCP = 65536, UST = 4160, UCH = 4 * UST;
constexpr int GT = 1088, GW = 5 * GT, GBUF = 8 * GW, YT2 = CNT + 256, YT2ROW = 168;
static_assert(W2U_G + 4096 <= KARR + 4 * KCH && UCP + 4 * UCH <= CNT && 3 * GBUF <= CNT && YT2 + 64 * YT2ROW <= LDS_BYTES, "LDS map");
#define NB1 24
#define WARM 0
#define LAUX 16
constexpr unsigned SPIN_CAP = 1u << 17;
#define SLP 1

DI float bflo(unsigned v) { return __uint_as_float(v << 16); }
DI float bfhi(unsigned v) { return __uint_as_float(v & 0xffff0000u); }
DI f2 unpk(unsigned v) { return f2{bflo(v), bfhi(v)}; }
DI unsigned pk2(f2 p) { return __builtin_bit_cast(unsigned, __builtin_convertvector(p, bf2)); }
DI unsigned pk(float a, float b) { return pk2(f2{a, b}); }
DI float rbf(float a) { return bflo(pk(a, a)); }
DI bf8 asbf(v4u v) { return __builtin_bit_cast(bf8, v); }
DI v4u mk_srd(const void* p, unsigned bytes) { u64 a = (u64)p; return v4u{(unsigned)a, (unsigned)(a >> 32) & 0xffffu, bytes, 0x00020000u}; }
#define SET_M0(v) asm volatile("s_mov_b32 m0, %0\n\ts_nop 0" ::"s"(__builtin_amdgcn_readfirstlane(v)) : "memory")
#define DMA(srd, voff, soff, imm) asm volatile("buffer_load_dwordx4 %0, %1, %2 offen offset:" #imm " lds" ::"v"(voff), "s"(srd), "s"(soff) : "memory")
#define WAITV(n) asm volatile("s_waitcnt vmcnt(" #n ")" ::: "memory")
#define PIN() __builtin_amdgcn_sched_barrier(0)
#define ORDER() asm volatile("" ::: "memory")
DI void bar() { asm volatile("s_waitcnt lgkmcnt(0)\n\ts_barrier" ::: "memory"); }
DI void bar_raw() { PIN(); asm volatile("s_barrier" ::: "memory"); PIN(); }
DI v2u trd(LAS char* p) { return __builtin_bit_cast(v2u, __builtin_amdgcn_ds_read_tr16_b64_v4i16((LAS s4*)p)); }
DI v4u trd2(LAS char* p0, LAS char* p1) { v2u a = trd(p0), b = trd(p1); return v4u{a.x, a.y, b.x, b.y}; }
DI float dot2(unsigned a, unsigned b, float c) { return __builtin_amdgcn_fdot2_f32_bf16(__builtin_bit_cast(bf2, a), __builtin_bit_cast(bf2, b), c, false); }
DI float dot8(v4u a, v4u b, float c) { return dot2(a.w, b.w, dot2(a.z, b.z, dot2(a.y, b.y, dot2(a.x, b.x, c)))); }
DI float sum8(v4u a, float c) { const unsigned o = 0x3f803f80u; return dot2(a.w, o, dot2(a.z, o, dot2(a.y, o, dot2(a.x, o, c)))); }
DI f2 fma2(f2 a, f2 b, f2 c) { return __builtin_elementwise_fma(a, b, c); }
struct cm { f2 p, n; };
DI cm mkm(f2 b) { return cm{b, f2{-b.y, b.x}}; }
DI f2 cmul(f2 a, cm b) { return fma2(a.yy, b.n, a.xx * b.p); }
DI f2 cexpl(float a, float th, float l) {
  const float m = __builtin_amdgcn_exp2f(a * l), rv = __builtin_amdgcn_fractf(th * l);
  return f2{m, m} * f2{__builtin_amdgcn_cosf(rv), __builtin_amdgcn_sinf(rv)};
}
DI float xor32(float v) { const auto r = __builtin_amdgcn_permlane32_swap(__float_as_uint(v), __float_as_uint(v), false, false); return __uint_as_float(r[0]) + __uint_as_float(r[1]); }
DI void st16(__amdgpu_buffer_rsrc_t r, v4u v, unsigned off) { __builtin_amdgcn_raw_buffer_store_b128(v, r, off, 0, 16); }
DI v4u ld16(__amdgpu_buffer_rsrc_t r, unsigned voff, unsigned soff) { return __builtin_amdgcn_raw_buffer_load_b128(r, voff, soff, LAUX); }
DI void put_flag(char* ws, int k, u64 tag) { __hip_atomic_store((GAS u64*)(ws + WS_FLAG + 128u * k), tag, __ATOMIC_RELAXED, __HIP_MEMORY_SCOPE_AGENT); }
DI u64 ld_word(char* ws, unsigned off) { return __hip_atomic_load((GAS u64*)(ws + off), __ATOMIC_RELAXED, __HIP_MEMORY_SCOPE_AGENT); }
DI void wait_flags(char* ws, int k, u64 tag) {
#pragma unroll 1
  for (unsigned s = 0; s < SPIN_CAP; ++s) {
    if (__all(ld_word(ws, WS_FLAG + 128u * k) == tag)) break;
    __builtin_amdgcn_s_sleep(1);
  }
  __builtin_amdgcn_fence(__ATOMIC_ACQUIRE, "wavefront");
}
DI u64 next_tag(u64 ep) { return ep * 6364136223846793005ull + 1442695040888963407ull; }
DI float ld_bf_uniform(const u16* p, int i) { const unsigned v = ((const unsigned*)p)[i >> 1]; return (i & 1) ? bfhi(v) : bflo(v); }
DI void lds_put(unsigned addr, unsigned v) { asm volatile("s_mov_b64 exec, 1\n\tds_write_b32 %0, %1\n\ts_mov_b64 exec, -1" ::"v"(addr), "v"(v) : "memory"); }
DI bool lds_arrive(LAS char* S, int c, int q) {
  asm volatile("s_waitcnt lgkmcnt(0)" ::: "memory");
  lds_put(CNT + 16 * c + 4 * q, 1u);
  const v4u v = *(volatile LAS v4u*)(S + CNT + 16 * c);
  return __builtin_amdgcn_readfirstlane(v.x & v.y & v.z & v.w);
}
DI void lds_wait(LAS char* S, int c) {
#pragma unroll 1
  for (unsigned s = 0; s < (SPIN_CAP << 4); ++s) {
    const v4u v = *(volatile LAS v4u*)(S + CNT + 16 * c);
    if (__builtin_amdgcn_readfirstlane(v.x & v.y & v.z & v.w)) break;
    __builtin_amdgcn_s_sleep(SLP);
  }
  ORDER();
}
DI void lds_set(LAS char* S, int off, unsigned v) { asm volatile("s_waitcnt lgkmcnt(0)" ::: "memory"); lds_put(off, v); }
DI unsigned lds_get(LAS char* S, unsigned off) { return *(volatile LAS unsigned*)(S + off); }
DI void lds_wait_ge(LAS char* S, unsigned off, unsigned n) {
#pragma unroll 1
  for (unsigned s = 0; s < (SPIN_CAP << 4); ++s) {
    if (__all(lds_get(S, off) >= n)) break;
    __builtin_amdgcn_s_sleep(SLP);
  }
  ORDER();
}
DI u64 rd_tag(LAS char* S) { const v2u t = *(volatile LAS v2u*)(S + TAGL); return ((u64)(unsigned)__builtin_amdgcn_readfirstlane(t.y) << 32) | (unsigned)__builtin_amdgcn_readfirstlane(t.x); }
DI u64 get_tag(LAS char* S) { lds_wait_ge(S, TAGL + 8, 1); return rd_tag(S); }
DI u64 ld_hidden(char* ws, unsigned off) { u64 v; asm volatile("global_load_dwordx2 %0, %1, %2 sc1" : "=v"(v) : "v"(off), "s"(ws) : "memory"); return v; }
DI float sadd(float a, float b) { float r; asm("v_add_f32 %0, %1, %2" : "=v"(r) : "v"(a), "v"(b)); return r; }
DI float smul(float a, float b) { float r; asm("v_mul_f32 %0, %1, %2" : "=v"(r) : "v"(a), "v"(b)); return r; }
DI float ssub(float a, float b) { float r; asm("v_sub_f32 %0, %1, %2" : "=v"(r) : "v"(a), "v"(b)); return r; }
DI bool tag_ok(u64 g, u64 tag) { return __all((unsigned)(g >> 32) == (unsigned)tag); }
DI u64 wait_granule(char* ws, unsigned off, u64 tag) {
  u64 g = 0;
#pragma unroll 1
  for (unsigned s = 0; s < SPIN_CAP; ++s) { g = ld_word(ws, off); if (tag_ok(g, tag)) break; }
  return g;
}
#define GOT(v) asm volatile("" : "+v"(v))
DI void check_flags(char* ws, int k, u64 seen, u64 tag) {
  if (!__all(seen == tag)) wait_flags(ws, k, tag);
  __builtin_amdgcn_fence(__ATOMIC_ACQUIRE, "wavefront");
}
#define STAMP(k)
#define STAMP_DECL
#define STAMP_OUT()

__global__ __launch_bounds__(512, 2) void s4mlp_kernel(const u16* __restrict__ x, const u16* __restrict__ Are, const u16* __restrict__ Aim,
                                                      const u16* __restrict__ Bre, const u16* __restrict__ Bim, const u16* __restrict__ Cre,
                                                      const u16* __restrict__ Cim, const u16* __restrict__ b1, const u16* __restrict__ logdt,
                                                      const u16* __restrict__ dskip, const u16* __restrict__ w1, const u16* __restrict__ w2,
                                                      const u16* __restrict__ b2, u16* __restrict__ out, char* ws) {
  LAS char* const S = (LAS char*)0;
  const int tid = threadIdx.x, lane = tid & 63, w = __builtin_amdgcn_readfirstlane(tid >> 6), wq = w & 3;
  const int b = blockIdx.x;
  STAMP_DECL;
  STAMP(0);
  v4u pf0 = {}, pf1 = {};
  if (w == 7) {
    unsigned len;
    asm volatile("s4_code_begin:\n\ts_mov_b32 %0, s4_code_end-s4_code_begin-256" : "=s"(len));
    const v4u sc = mk_srd((const void*)__builtin_amdgcn_s_getpc(), __builtin_amdgcn_readfirstlane(len));
    asm volatile("s_nop 4\n\tbuffer_load_dwordx4 %0, %2, %3, 0 offen\n\tbuffer_load_dwordx4 %1, %4, %3, 0 offen"
                 : "=&v"(pf0), "=&v"(pf1) : "v"((b >> 3) * 1024 + lane * 16), "s"(sc), "v"((b >> 3) * 1024 + lane * 16 + 32768) : "memory");
    *(LAS unsigned*)(S + CNT + 4 * lane) = 0u;
  }
  asm volatile("" ::"s"(x), "s"(w1), "s"(ws), "s"(Are), "s"(Aim), "s"(Bre), "s"(Bim), "s"(Cre), "s"(Cim), "s"(w2), "s"(logdt), "s"(dskip));
  bar();
#define IDS()                                                                                                              \
  int bl = b;                                                                                                              \
  asm volatile("" : "+s"(bl));                                                                                             \
  bl = __builtin_amdgcn_readfirstlane(bl);                                                                                 \
  const int xcd = bl & 7, bi = bl >> 3;                                                                                    \
  const int rp = 4 * (xcd >> 1) + (bi >> 3), cb = 7 * (xcd & 1) + (bi & 7);                                 \
  const bool spare = (bi & 7) == 7;                                         \
  const int c0 = cb * 128 > F - 128 ? F - 128 : cb * 128, gi = rp * 14 + cb, sp = xcd * 4 + (bi >> 3);                      \
  const int yi = spare ? 32 * (sp >> 2) + 28 + (sp & 3) : 32 * (gi / 28) + gi % 28;                                        \
  const int ch00 = spare ? 80 * (sp >> 2) + 56 + 6 * (sp & 3) : 80 * (gi / 28) + 2 * (gi % 28), nbat = spare ? 3 : 1;      \
  const int rt = 8 * (xcd >> 1) + (bi >> 2), ct = 4 * (xcd & 1) + (bi & 3);                                 \
  const int kar = spare ? KARR : KARR + 4 * KCH;                                                                             \
  const int s0 = (cb * 3 + rp * 5 + (rp >> 1)) % 10;             \
  const unsigned w2s = WS_W2T + (ct * 270 + w) * 1024;                                                                      \
  const int fhk = FL_H + (rt >> 1) * 14 + (lane < 14 ? lane : 0);                                                          \
  (void)c0; (void)yi; (void)ch00; (void)nbat; (void)kar; (void)s0; (void)w2s; (void)fhk
  const __amdgpu_buffer_rsrc_t rws = __builtin_amdgcn_make_buffer_rsrc(ws, 0, WS_END, 0x00020000);
  const int h = lane >> 5, g = lane >> 4, q = (lane & 15) >> 2, p = lane & 3, blk = g & 1;
  auto fw_flag = [&](int ct) { const int k = (lane >> 1) * 8 + w; return FL_W + (k < 54 ? k : w) * 10 + ((ct * 80 + (lane & 1) * 79) >> 6); };
  const unsigned k6 = w < 6 ? 49152u : 0u;
  v4u wf2[35];
  const unsigned w2v = lane * 16, w2v6 = w2v + (w < 6 ? 0u : OOB);
  auto load_w2_ = [&](unsigned w2s, int i0, int i1) {
#pragma unroll
    for (int i = 0; i < 35; ++i)
      if (i >= i0 && i < i1) wf2[i] = ld16(rws, i >= 30 ? w2v6 : w2v, w2s + (i % 5) * (54 * 1024) + (i / 5) * 8192);
  };
  auto warm_w2 = [&](int c2, u64 tag) {
    wait_flags(ws, fw_flag(c2), tag);
    load_w2_(WS_W2T + (c2 * 270 + w) * 1024, 0, 35);
#pragma unroll
    for (int i = 0; i < 35; ++i) GOT(wf2[i]);
  };
  const int tt = tid & 255;
  v2u gr[8];
  v4u un[2];
  auto load_stats = [&]() {
#pragma unroll
    for (int i = 0; i < 8; ++i) gr[i] = __builtin_amdgcn_raw_buffer_load_b64(rws, tt * 8, WS_ST + i * 2048, 16);
  };
  auto check_stats = [&](u64 tag) {
    unsigned bad = 0;
#pragma unroll
    for (int i = 0; i < 8; ++i) bad |= gr[i].y ^ (unsigned)tag;
    if (!__all(bad == 0)) {
#pragma unroll
      for (int i = 0; i < 8; ++i) gr[i].x = (unsigned)wait_granule(ws, WS_ST + i * 2048 + tt * 8, tag);
    }
  };
  auto load_u = [&](int ch) {
#pragma unroll
    for (int j = 0; j < 2; ++j) un[j] = ld16(rws, tt * 16, WS_XT + (ch + j) * 4096);
  };
  auto prep = [&](int bt) {
    float rr[8];
#pragma unroll
    for (int e = 0; e < 8; ++e) rr[e] = __builtin_amdgcn_rcpf(bfhi(gr[e].x));
    asm volatile("s_nop 0" : "+v"(rr[0]), "+v"(rr[1]), "+v"(rr[2]), "+v"(rr[3]), "+v"(rr[4]), "+v"(rr[5]), "+v"(rr[6]), "+v"(rr[7]));
#pragma unroll
    for (int e = 0; e < 4; ++e) {
      const float n0 = bflo(gr[2 * e].x), n1 = bflo(gr[2 * e + 1].x);
#pragma unroll
      for (int j = 0; j < 2; ++j) un[j][e] = pk(smul(ssub(bflo(un[j][e]), n0), rr[2 * e]), smul(ssub(bfhi(un[j][e]), n1), rr[2 * e + 1]));
    }
    LAS char* U = S + UCP + (bt & 1) * (2 * UCH) + 32 + tt * 16;
    if (bt >= 2) lds_wait_ge(S, CNT + 16 * 3 + 4 * p, bt - 1);
    if (tt < 16) *(LAS v4u*)(U - 32 - tt * 16 + (tt >> 1) * UST + (tt & 1) * 16) = v4u{0u, 0u, 0u, 0u};
    *(LAS v4u*)U = un[0];
    *(LAS v4u*)(U + UCH) = un[1];
    lds_put(CNT + 16 * 7 + 4 * wq, bt + 1);
    lds_wait_ge(S, CNT + 16 * 7 + 4 * p, bt + 1);
#pragma unroll
    for (int j = 0; j < 2; ++j) {
      const v4u d = un[j]; const v2u pv = *(LAS v2u*)(U + j * UCH - 8);
      const unsigned a0 = __builtin_amdgcn_alignbit(pv.y, pv.x, 16), a1 = __builtin_amdgcn_alignbit(d.x, pv.y, 16), a2 = __builtin_amdgcn_alignbit(d.y, d.x, 16),
                     a3 = __builtin_amdgcn_alignbit(d.z, d.y, 16), a4 = __builtin_amdgcn_alignbit(d.w, d.z, 16);
      *(LAS v4u*)(U + j * UCH + UST) = v4u{a1, a2, a3, a4};
      *(LAS v4u*)(U + j * UCH + 2 * UST) = v4u{pv.y, d.x, d.y, d.z};
      *(LAS v4u*)(U + j * UCH + 3 * UST) = v4u{a0, a1, a2, a3};
    }
    lds_put(CNT + 16 * 8 + 4 * wq, bt + 1);
  };
  u64 tag, fh = 0;
  if (w >= 4) {
    IDS();
    int sn = s0;
    const int u0 = spare ? 448 + 3 * sp : 2 * gi;
    const v4u sx = mk_srd(x, L * D * 2), sw1 = mk_srd(w1, D * F * 2);
    unsigned vx[4], vw[4];
    auto ring_setup = [&]() {
      int ln = lane;
      asm volatile("" : "+v"(ln));
#pragma unroll
      for (int j = 0; j < 4; ++j) {
        const int rx = 8 * j + (ln >> 3);
        vx[j] = (rp * 128 + rx) * (D * 2) + 16 * ((ln & 7) ^ ((rx >> 1) & 7)) - 1024 * j;
        vw[j] = (4 * j + (ln >> 4)) * (F * 2) + c0 * 2 + 16 * ((ln & 15) ^ (((ln >> 4) << 2) | j)) - 1024 * j;
      }
    };
    auto quarter = [&](int st, int slot, int k) {
      const unsigned so = st * 128 + k * (32 * D * 2), sv = st * (64 * F * 2) + k * (16 * F * 2);
      SET_M0(slot * SLOT + k * 4096);
      DMA(sx, vx[0], so, 0); DMA(sx, vx[1], so, 1024); DMA(sx, vx[2], so, 2048); DMA(sx, vx[3], so, 3072);
      SET_M0(slot * SLOT + 16384 + k * 4096);
      DMA(sw1, vw[0], sv, 0); DMA(sw1, vw[1], sv, 1024); DMA(sw1, vw[2], sv, 2048); DMA(sw1, vw[3], sv, 3072);
    };
    if (w == 7) {
      const __amdgpu_buffer_rsrc_t rx = __builtin_amdgcn_make_buffer_rsrc((void*)x, 0, L * D * 2, 0x00020000),
                                   rw2 = __builtin_amdgcn_make_buffer_rsrc((void*)w2, 0, D * F * 2, 0x00020000);
      const u64 ep = ld_word(ws, 0);
      v4u xv[10], wv[3][4];
#pragma unroll
      for (int i = 0; i < 10; ++i) xv[i] = __builtin_amdgcn_raw_buffer_load_b128(rx, (lane >> 3) * (D * 2) + (lane & 7) * 16 + i * 128, b * (8 * D * 2), 0);
#pragma unroll
      for (int k = 0; k < 3; ++k) {
        const int u = u0 + k;
        const unsigned vo = (lane >> 3) * (D * 2) + (lane & 7) * 16 + ((k < 2 || spare) && u < 540 ? 0u : OOB);
#pragma unroll
        for (int i = 0; i < 4; ++i) wv[k][i] = __builtin_amdgcn_raw_buffer_load_b128(rw2, vo, ((u / 10) * 32 + 8 * i) * (D * 2) + (u % 10) * 128, 0);
      }
      PIN();
      ring_setup();
      const u64 tg = next_tag(ep);
      lds_put(TAGL, (unsigned)tg); lds_put(TAGL + 4, (unsigned)(tg >> 32));
      lds_set(S, TAGL + 8, 1);
      GOT(pf0); GOT(pf1);
      float sm[2] = {}, sq[2] = {};
#pragma unroll
      for (int i = 0; i < 10; ++i) { sm[i & 1] = sum8(xv[i], sm[i & 1]); sq[i & 1] = dot8(xv[i], xv[i], sq[i & 1]); }
      float sum = sm[0] + sm[1], ssq = sq[0] + sq[1];
#pragma unroll
      for (int m = 1; m < 8; m <<= 1) { sum += __shfl_xor(sum, m); ssq += __shfl_xor(ssq, m); }
      const float mean = sum * (1.f / D), var = fmaxf(ssq * (1.f / D) - mean * mean, 0.f);
      const unsigned ms = pk(mean, __builtin_amdgcn_sqrtf(rbf(rbf(var) + 1e-5f)));
      if ((lane & 7) == 0) __hip_atomic_store((GAS u64*)(ws + WS_ST + (lane >> 3) * 2048 + b * 8), (tg << 32) | ms, __ATOMIC_RELAXED, __HIP_MEMORY_SCOPE_AGENT);
      STAMP(1);
      LAS char* U = S + (spare ? W2U_S : W2U_G);
      LAS char* B = U + (4 * (blk + 4 * (g >> 1)) + q) * 128 + 8 * p;
      GOT(wv[2][0]); GOT(wv[2][1]); GOT(wv[2][2]); GOT(wv[2][3]);
#pragma unroll
      for (int k = 0; k < 3; ++k) {
        const int u = u0 + k;
        if ((k < 2 || spare) && u < 540) {
#pragma unroll
          for (int i = 0; i < 4; ++i) *(LAS v4u*)(U + i * 1024 + lane * 16) = wv[k][i];
#pragma unroll
          for (int n = 0; n < 4; ++n) st16(rws, trd2(B + 32 * n, B + 32 * n + 1024), lane * 16 + WS_W2T + (((u % 10) * 4 + n) * 54 + u / 10) * 1024);
        }
      }
    } else if (!spare) {
      ring_setup();
#pragma unroll 1
      for (int t = wq; t < 12; t += 3) {
        if (t >= 9) { WAITV(8); lds_put(CNT + 28, 1); lds_put(CNT + 16 + 4 * wq, 1); }
        quarter(sn + (t >> 2) >= 10 ? sn + (t >> 2) - 10 : sn + (t >> 2), t >> 2, t & 3);
      }
      WAITV(8);
      STAMP(1);
    } else if (sp < 27) {
      const __amdgpu_buffer_rsrc_t rw1 = __builtin_amdgcn_make_buffer_rsrc((void*)w1, 0, D * F * 2, 0x00020000);
      v4u v[27];
#pragma unroll
      for (int i = 0; i < 27; ++i) v[i] = __builtin_amdgcn_raw_buffer_load_b128(rw1, (216 * wq + 8 * i + (lane >> 3)) * (F * 2) + (lane & 7) * 16, sp * 128, 0);
      PIN();
      f2 a[4] = {};
#pragma unroll
      for (int i = 0; i < 27; ++i)
#pragma unroll
        for (int e = 0; e < 4; ++e) a[e] += unpk(v[i][e]);
      *(LAS f4*)(S + (wq * 64 + lane) * 32) = f4{a[0].x, a[0].y, a[1].x, a[1].y};
      *(LAS f4*)(S + (wq * 64 + lane) * 32 + 16) = f4{a[2].x, a[2].y, a[3].x, a[3].y};
      lds_put(CNT + 16 + 12, 1);
      if (lds_arrive(S, 1, wq)) {
        float t = 0.f;
#pragma unroll
        for (int i = 0; i < 24; ++i) t += *(LAS float*)(S + i * 256 + lane * 4);
        __hip_atomic_store((GAS u64*)(ws + WS_CS + (sp * 64 + lane) * 8), (get_tag(S) << 32) | __float_as_uint(t), __ATOMIC_RELAXED, __HIP_MEMORY_SCOPE_AGENT);
      }
    }
    int fwk = fw_flag(ct);
    GOT(fwk);
    STAMP(2);
    u64 pw = 0, px = 0;
    const int fxk = FL_X + (lane & 15) * 10 + ((ch00 + ((lane & 16) ? 2 * nbat - 1 : 0)) >> 6);
    if (!spare) {
      bar();
      STAMP(6);
      tag = rd_tag(S);
      sn = sn >= 7 ? sn - 7 : sn + 3;
#pragma unroll 1
      for (int n = 0; n < 6; ++n) {
        quarter(sn, (n + 3) & 3, wq);
        sn = sn == 9 ? 0 : sn + 1;
        WAITV(8);
        if (n == 0 && w == 7) { if (lane < 2) put_flag(ws, FL_W + u0 + lane, tag); }
        bar_raw();
      }
      quarter(sn, 1, wq);
      pw = ld_hidden(ws, WS_FLAG + 128u * fwk); px = ld_hidden(ws, WS_FLAG + 128u * fxk);
      load_stats();
      WAITV(18);
      bar_raw();
      STAMP(4);
      WAITV(8);
      GOT(pw); GOT(px);
      check_flags(ws, fxk, px, tag);
      load_u(ch00);
      ORDER();
      bar_raw();
      STAMP(5);
      check_flags(ws, fwk, pw, tag);
    } else {
      tag = get_tag(S);
      if (w == 7) {
        WAITV(0);
        if (lane < 3 && u0 + lane < 540) put_flag(ws, FL_W + u0 + lane, tag);
      }
      if (WARM && (bi >> 3) != 3) warm_w2(4 * (xcd & 1) + (bi >> 3), tag);
      load_stats();
      wait_flags(ws, fwk, tag);
    }
    load_w2_(w2s, 0, NB1);
    PIN();
    if (spare) {
      wait_flags(ws, fxk, tag);
      load_u(ch00);
      GOT(un[0]); GOT(un[1]);
      bar_raw();
    }
    STAMP(1);
    check_stats(tag);
    prep(0);
    STAMP(12);
    LAS char* Kp = S + kar + 512 + 32 * ((lane & 15) - (g >> 1)) + 16 * blk - 64 * wq - 256;
    LAS char* const Up0 = S + UCP + q * UST + 32 + 32 * (g >> 1) - 16 * blk + 8 * p + 64 * wq - 8;
    LAS char* const Pp0 = S + wq * 8192 + g * 256 + (((lane & 15) ^ (2 * g)) * 16);
#pragma unroll 1
    for (int cj = 0; cj < 2 * nbat; ++cj) {
      const int bt = cj >> 1;
      if (!(cj & 1)) lds_wait_ge(S, CNT + 16 * 8 + 4 * p, bt + 1);
      LAS char* Up = Up0 + (cj & 3) * UCH;
      if (cj == 2 * nbat - 1) fh = ld_hidden(ws, WS_FLAG + 128u * fhk);
      f4 acc[8];
      v4u kf[8], kg[8], ua[2], ub[2];
      auto rd_u = [&](int s) { ua[s & 1] = trd2(Up + 512 * s + 8, Up + 512 * s); ub[s & 1] = trd2(Up + 512 * (7 - s) + 264, Up + 512 * (7 - s) + 256); kg[s] = *(LAS v4u*)(Kp + 512 * s); };
      rd_u(0);
#pragma unroll
      for (int e = 7; e >= 0; --e) kf[e] = *(LAS v4u*)(Kp + 512 * e + 256);
      if (cj == 0) {
        lds_wait(S, 6);
        STAMP(13);
      }
      PIN();
#pragma unroll
      for (int s = 0; s < 8; ++s) {
        if (s < 7) rd_u(s + 1);
#pragma unroll
        for (int R = 7; R >= s; --R) acc[R] = __builtin_amdgcn_mfma_f32_16x16x32_bf16(asbf(ua[s & 1]), asbf(kf[R - s]), s ? acc[R] : f4{0.f, 0.f, 0.f, 0.f}, 0, 0, 0);
#pragma unroll
        for (int R = 7 - s; R < 8; ++R) acc[R] = __builtin_amdgcn_mfma_f32_16x16x32_bf16(asbf(ub[s & 1]), asbf(kg[R - 7 + s]), acc[R], 0, 0, 0);
#pragma unroll
        for (int i = 0; i < 5; ++i) { __builtin_amdgcn_sched_group_barrier(0x8, 1, 0); __builtin_amdgcn_sched_group_barrier(0x80, 1, 0); }
        PIN();
      }
      if (bt) lds_wait_ge(S, CNT + 16 * 9 + 4 * p, cj - 1);
#pragma unroll
      for (int R = 0; R < 8; ++R) *(LAS f4*)(Pp0 + (cj & 1) * SLOT + R * 1024) = acc[R];
      lds_put(CNT + 16 * (2 + (cj & 1)) + 4 * wq, bt + 1);
      Kp += KCH;
      if (cj == 1 && spare) STAMP(2);
    }
    STAMP(7);
    WAITV(0);
    load_w2_(w2s, NB1, 35);
  } else {
    IDS();
    const int wf = w >> 1, tg = w & 1;
    unsigned xa[4], wa[2][2], pn[6];
    unsigned lgn, dskn;
    auto fetch = [&](int bt) {
      const int ch = ch00 + 2 * bt + wf, o = ch * N + tg * 64 + lane;
      pn[0] = Are[o]; pn[1] = Aim[o]; pn[2] = Bre[o]; pn[3] = Bim[o]; pn[4] = Cre[o]; pn[5] = Cim[o];
      lgn = logdt[ch]; dskn = dskip[ch];
      PIN();
    };
    fetch(0);
    int fwk = fw_flag(ct);
    GOT(fwk);
#pragma unroll
    for (int i = 0; i < 4; ++i) {
      const int row = 64 * tg + (lane & 31), c = 2 * wf + (i >> 1), t2 = i & 1;
      xa[i] = (row * 128 + 16 * (h ^ ((row >> 1) & 7))) ^ (32 * i);
      wa[i >> 1][i & 1] = 16384 + 256 * (8 * h + 4 * t2 + q) + 64 * (c ^ q) + 16 * ((2 * blk + (p >> 1)) ^ (2 * h + t2)) + 8 * (p & 1);
      GOT(xa[i]); GOT(wa[i >> 1][i & 1]);
    }
    PIN();
#pragma unroll 1
    for (int bt = 0; bt < nbat; ++bt) {
      const unsigned p0 = pn[0], p1 = pn[1], p2 = pn[2], p3 = pn[3], p4 = pn[4], p5 = pn[5];
      const float lg = bflo(lgn), dsk = bflo(dskn);
      if (bt + 1 < nbat) fetch(bt + 1);
      if (bt) lds_wait_ge(S, CNT + 16 * 5 + 4 * (w ^ 1), bt);
      LAS unsigned* T = (LAS unsigned*)(S + TAB + w * TW) + lane;
      LAS char* Tr = S + TAB + w * TW + g * 16 + (lane & 15) * TROW;
      constexpr int R = TROW / 4;
      const float dt = rbf(__expf(lg)), a = dt * bflo(p0) * 1.4426950408889634f;
      float th = dt * bflo(p1) * 0.15915494309189535f;
      th -= __builtin_rintf(th);
      const f2 e1 = cexpl(a, th, 1.f), e128 = cexpl(a, th, 128.f), e8 = cexpl(a, -th, 8.f);
      const cm m1 = mkm(e1), m128 = mkm(e128), m8 = mkm(e8), m16 = mkm(cmul(e8, m8));
      const f2 e2 = cmul(e1, m1);
      const cm m2 = mkm(e2), m4 = mkm(cmul(e2, m2));
      f2 y = cmul(f2{bflo(p4), -bflo(p5)}, mkm(f2{dt, dt} * f2{bflo(p2), -bflo(p3)})), y2 = cmul(y, m8), t = cexpl(a, th, 1024.f);
#pragma unroll
      for (int i = 0; i < 8; ++i) {
        T[(2 * i) * R] = pk2(y); T[(2 * i + 1) * R] = pk2(y2); T[(16 + i) * R] = pk2(t);
        if (i < 7) { y = cmul(y, m16); y2 = cmul(y2, m16); }
        t = cmul(t, m128);
      }
      T[24 * R] = pk2(t);
      v4u fy[4], fa[4][2];
      f4 ka[3] = {};
#pragma unroll
      for (int ks = 0; ks < 4; ++ks) { fy[ks] = *(LAS v4u*)(Tr + ks * 64); fa[ks][0] = *(LAS v4u*)(Tr + 16 * TROW + ks * 64); }
      PIN();
      f2 u = {1.f, 0.f}, v = e128, z = cmul(e128, m128);
#pragma unroll
      for (int i = 0; i < 8; ++i) {
        T[i * R] = pk2(u); T[(8 + i) * R] = pk2(v);
        if (i < 7) { u = cmul(u, m1); v = cmul(v, m1); }
        if (i < 2) {
          const f2 z2 = cmul(z, m2);
          T[(16 + 4 * i) * R] = pk2(z); T[(17 + 4 * i) * R] = pk2(z2); T[(18 + 4 * i) * R] = pk2(cmul(z, m4)); T[(19 + 4 * i) * R] = pk2(cmul(z2, m4));
        } else if (i < 6) {
          T[(20 + 2 * i) * R] = pk2(z); T[(21 + 2 * i) * R] = pk2(cmul(z, m4));
        }
        if (i < 5) z = cmul(z, m128);
      }
      PIN();
#pragma unroll
      for (int ks = 0; ks < 4; ++ks) ka[2] = __builtin_amdgcn_mfma_f32_16x16x32_bf16(asbf(fa[ks][0]), asbf(fy[ks]), ka[2], 0, 0, 0);
#pragma unroll
      for (int ks = 0; ks < 4; ++ks) { fa[ks][0] = *(LAS v4u*)(Tr + ks * 64); fa[ks][1] = *(LAS v4u*)(Tr + 16 * TROW + ks * 64); }
      PIN();
#pragma unroll
      for (int ks = 0; ks < 4; ++ks)
#pragma unroll
        for (int i = 0; i < 2; ++i) ka[i] = __builtin_amdgcn_mfma_f32_16x16x32_bf16(asbf(fa[ks][i]), asbf(fy[ks]), ka[i], 0, 0, 0);
      if (bt == 0) STAMP(1);
      {
        LAS char* X = S + TAB + w * TW + 4608 + lane * 16;
        *(LAS f4*)X = tg ? ka[0] : ka[1];
        *(LAS f4*)(X + 1024) = ka[2];
        lds_put(CNT + 16 * 4 + 4 * w, bt + 1);
        lds_wait_ge(S, CNT + 16 * 4 + 4 * (w ^ 1), bt + 1);
        LAS char* Y = S + TAB + (w ^ 1) * TW + 4608 + lane * 16;
        f4 km = (tg ? ka[1] : ka[0]) + *(LAS f4*)Y, k2 = ka[2] + *(LAS f4*)(Y + 1024);
        GOT(km); GOT(k2);
        lds_put(CNT + 16 * 5 + 4 * w, bt + 1);
        const int c = lane & 15;
        LAS char* K = S + kar + (2 * bt + wf) * KCH;
        LAS u16* T1 = (LAS u16*)(S + TAB + w * TW);
        const unsigned q01 = pk2(k2.xy), q23 = pk2(k2.zw);
        if (!tg) {
          if (lane == 0) km.x += dsk;
          *(LAS v2u*)(K + 512 + 256 * (g >> 1) + 16 * c + 8 * blk) = v2u{pk2(km.xy), pk2(km.zw)};
          LAS u16* T3 = T1 + 64 * g + c;
          T3[0] = (u16)q01; T3[16] = (u16)(q01 >> 16); T3[32] = (u16)q23; T3[48] = (u16)(q23 >> 16);
          ORDER();
          const unsigned c0_ = T1[lane], c1 = T1[1 + lane], d0 = T1[64 + lane], d1 = T1[65 + lane];
          if (h) *(LAS v4u*)(K + 16 * (lane & 31)) = v4u{0u, 0u, 0u, 0u};
          *(LAS v4u*)(K + 2560 + 16 * lane) = v4u{c0_ * 0x10001u, c0_ * 0x10001u, ((lane & 1) ? c1 : c0_) | (c1 << 16), c1 * 0x10001u};
          *(LAS v4u*)(K + 3584 + 16 * lane) = v4u{d0 * 0x10001u, d0 * 0x10001u, ((lane & 1) ? d1 : d0) | (d1 << 16), d1 * 0x10001u};
        } else {
          const unsigned p01 = pk2(km.xy), p23 = pk2(km.zw);
          if (g < 2) *(LAS v2u*)(T1 + 64 * g + 4 * c) = v2u{p01, p23};
          else { *(LAS unsigned*)(T1 + 64 * g + 2 * c) = p01; *(LAS unsigned*)(T1 + 64 * g + 32 + 2 * c) = p23; }
          if (lane == 0) T1[256] = (u16)q01;
          ORDER();
          const v2u a1 = *(LAS v2u*)(T1 + 4 * (lane & 31));
          const unsigned a4 = T1[4 * (lane & 31) + 4], b0 = *(LAS unsigned*)(T1 + 128 + 2 * lane), b2_ = T1[130 + 2 * lane];
          if (!h) *(LAS v4u*)(K + 1024 + 16 * lane) = v4u{(a1.x & 0xffffu) * 0x10001u, (a1.x >> 16) | (a1.y << 16), (a1.y & 0xffffu) * 0x10001u, (a1.y >> 16) | (a4 << 16)};
          *(LAS v4u*)(K + 1536 + 16 * lane) = v4u{(b0 & 0xffffu) * 0x10001u, b0, (b0 >> 16) * 0x10001u, b2_ * 0x10001u};
        }
      }
    }
    if (cb < 10 && !spare) {
      lds_wait(S, 1);
      v2u xt[8];
#pragma unroll
      for (int m = 0; m < 8; ++m) {
        const int rr = 32 * g + 4 * m + q;
        xt[m] = trd(S + 128 * rr + 16 * ((2 * w + (p >> 1)) ^ ((rr >> 1) & 7)) + 8 * (p & 1));
      }
      LAS char* X = S + TAB + w * TW;
#pragma unroll
      for (int k = 0; k < 4; ++k) *(LAS v4u*)(X + (lane & 15) * 272 + g * 64 + k * 16) = v4u{xt[2 * k].x, xt[2 * k].y, xt[2 * k + 1].x, xt[2 * k + 1].y};
#pragma unroll
      for (int k = 0; k < 4; ++k)
        st16(rws, *(LAS v4u*)(X + (4 * k + g) * 272 + (lane & 15) * 16), WS_XT + (s0 * 64 + 16 * w + 4 * k + g) * 4096 + rp * 256 + (lane & 15) * 16);
    }
    auto publish = [&](int j) {
      lds_wait_ge(S, CNT + 16 * (2 + (j & 1)) + 4 * p, (j >> 1) + 1);
      const unsigned pa = (j & 1) * SLOT + (tid >> 5) * 1024 + (((tid >> 1) & 15) ^ (4 * (tid & 1))) * 16 + (tid & 1) * 512;
      f4 lo[4], hi[4], l, m;
#pragma unroll
      for (int v = 0; v < 4; ++v) { lo[v] = *(LAS f4*)(S + pa + v * 8192); hi[v] = *(LAS f4*)(S + (pa ^ 32u) + 256 + v * 8192); }
#pragma unroll
      for (int e = 0; e < 4; ++e) { l[e] = sadd(sadd(lo[0][e], lo[1][e]), sadd(lo[2][e], lo[3][e])); m[e] = sadd(sadd(hi[0][e], hi[1][e]), sadd(hi[2][e], hi[3][e])); }
      GOT(l); GOT(m);
      lds_put(CNT + 16 * 9 + 4 * wq, j + 1);
      st16(rws, v4u{pk2(l.xy), pk2(l.zw), pk2(m.xy), pk2(m.zw)}, tid * 16 + WS_YT + (ch00 + j) * 4096);
    };
    STAMP(2);
    if (!spare) {
      f16v acc[2][2] = {};
      v4u fx[4][2], fw[4][2];
      auto rd = [&](int set, int so, int ks) {
        fw[set][0] = trd2(S + wa[0][0] + so + 4096 * ks, S + wa[0][1] + so + 4096 * ks);
        fx[set][0] = *(LAS v4u*)(S + xa[ks] + so);
        fx[set][1] = *(LAS v4u*)(S + xa[ks] + so + 4096);
        fw[set][1] = trd2(S + wa[1][0] + so + 4096 * ks, S + wa[1][1] + so + 4096 * ks);
      };
      auto mm = [&](int set) {
#pragma unroll
        for (int i = 0; i < 4; ++i) acc[i >> 1][i & 1] = __builtin_amdgcn_mfma_f32_32x32x16_bf16(asbf(fw[set][i >> 1]), asbf(fx[set][i & 1]), acc[i >> 1][i & 1], 0, 0, 0);
#pragma unroll
        for (int i = 0; i < 3; ++i) { __builtin_amdgcn_sched_group_barrier(0x8, 1, 0); __builtin_amdgcn_sched_group_barrier(0x100, 2, 0); }
        __builtin_amdgcn_sched_group_barrier(0x8, 1, 0);
        PIN();
      };
      u64 fw_seen = 0, g0 = 0, g1 = 0, gc = 0;
      unsigned b1v = 0;
      const unsigned o0 = WS_ST + (lane & 7) * 2048 + (rp * 16 + 8 * tg + ((lane & 31) >> 3)) * 8, oc = WS_CS + (c0 + 64 * wf + lane) * 8;
      asm volatile("" : "+v"(acc[0][0]), "+v"(acc[0][1]), "+v"(acc[1][0]), "+v"(acc[1][1]));
      bar();
      STAMP(6);
      tag = rd_tag(S);
      rd(0, 0, 0); rd(1, 0, 1);
      PIN();
#pragma unroll 1
      for (int n = 0; n < 5; ++n) {
        if (n == 4) { fw_seen = ld_hidden(ws, WS_FLAG + 128u * fwk); g0 = ld_hidden(ws, o0); g1 = ld_hidden(ws, o0 + 32); gc = ld_hidden(ws, oc);
            asm volatile("global_load_ushort %0, %1, %2" : "=v"(b1v) : "v"((c0 + 64 * wf + lane) * 2), "s"(b1) : "memory"); }
        rd(2, 0, 2); mm(0);
        rd(3, 0, 3); mm(1);
        rd(0, SLOT, 0); mm(2);
        rd(1, SLOT, 1); mm(3);
        if (n == 4) {
          WAITV(0); GOT(fw_seen); GOT(g0); GOT(g1); GOT(gc); GOT(b1v);
          if (!tag_ok(gc, tag)) gc = wait_granule(ws, oc, tag);
          *(LAS unsigned*)(S + COLT + w * 512 + 256 + lane * 4) = b1v << 16;
          *(LAS unsigned*)(S + COLT + w * 512 + lane * 4) = (unsigned)gc;
          if (!tag_ok(g0, tag)) g0 = wait_granule(ws, o0, tag);
          if (!tag_ok(g1, tag)) g1 = wait_granule(ws, o0 + 32, tag);
          PIN();
        } else {
          if (n == 1 && w == 1 && cb < 10 && lane == 0) put_flag(ws, FL_X + rp * 10 + s0, tag);
          bar_raw();
        }
        rd(2, SLOT, 2); mm(0);
        rd(3, SLOT, 3); mm(1);
#pragma unroll
        for (int i = 0; i < 4; ++i) { xa[i] ^= 2 * SLOT; wa[i >> 1][i & 1] ^= 2 * SLOT; }
        rd(0, 0, 0); mm(2);
        rd(1, 0, 1); mm(3);
        if (n == 0) WAITV(0);
        if (n < 4) bar_raw();
      }
      STAMP(3);
      const unsigned hv = WS_HT + (((rp * 8 + 4 * tg + blk) * 54 + (c0 >> 5) + 2 * wf) * 64 + (lane & 15) + 16 * h) * 16 + ((cb == 13 && wf == 0) ? OOB : 0u);
#pragma unroll
      for (int c = 0; c < 2; ++c) {
        f4 cs[4], bb[4];
#pragma unroll
        for (int a = 0; a < 4; ++a) { LAS char* ct_ = S + COLT + w * 512 + (32 * c + 8 * a + 4 * h) * 4; cs[a] = *(LAS f4*)ct_; bb[a] = *(LAS f4*)(ct_ + 256); }
#pragma unroll
        for (int tt = 0; tt < 2; ++tt) {
          const float rs = __builtin_amdgcn_rcpf(bfhi((unsigned)(tt ? g1 : g0))), mr = -bflo((unsigned)(tt ? g1 : g0)) * rs;
          unsigned o[8];
#pragma unroll
          for (int a = 0; a < 4; ++a) {
#pragma unroll
            for (int e = 0; e < 2; ++e) {
              const f2 v = fma2(f2{acc[c][tt][4 * a + 2 * e], acc[c][tt][4 * a + 2 * e + 1]}, f2{rs, rs}, fma2(f2{mr, mr}, e ? cs[a].zw : cs[a].xy, e ? bb[a].zw : bb[a].xy));
              const f2 z = v * fma2(v * v, f2{-0.10294324f, -0.10294324f}, f2{-2.30220819f, -2.30220819f});
              const f2 dn = f2{__builtin_amdgcn_exp2f(z.x), __builtin_amdgcn_exp2f(z.y)} + f2{1.f, 1.f};
              o[2 * a + e] = pk2(v * f2{__builtin_amdgcn_rcpf(dn.x), __builtin_amdgcn_rcpf(dn.y)});
            }
          }
          st16(rws, v4u{o[0], o[1], o[2], o[3]}, hv + (tt * 108 + c) * 1024);
          st16(rws, v4u{o[4], o[5], o[6], o[7]}, hv + (tt * 108 + c) * 1024 + 512);
          PIN();
        }
      }
      STAMP(4);
      lds_put(CNT + 16 * 6 + 4 * wq, 1);
      check_flags(ws, fwk, fw_seen, tag);
      load_w2_(w2s, 0, 8);
      WAITV(8);
      if (lds_arrive(S, 0, wq) && lane == 0) put_flag(ws, FL_H + gi, tag);
      STAMP(5);
    } else {
      lds_put(CNT + 16 * 6 + 4 * wq, 1);
      load_stats();
      bar();
      tag = rd_tag(S);
      check_stats(tag);
#pragma unroll 1
      for (int bt = 1; bt < 3; ++bt) {
        load_u(ch00 + 2 * bt);
        if (bt == 1) lds_wait_ge(S, CNT + 16 * 8 + 4 * p, 1);
        prep(bt);
        publish(2 * bt - 2);
        publish(2 * bt - 1);
      }
      wait_flags(ws, fwk, tag);
      load_w2_(w2s, 0, 8);
    }
    load_w2_(w2s, 8, 35);
    publish(2 * nbat - 2);
    fh = ld_hidden(ws, WS_FLAG + 128u * fhk);
    publish(2 * nbat - 1);
    STAMP(7);
    WAITV(1);
  }
  {
    IDS();
    GOT(fh);
    check_flags(ws, fhk, fh, tag);
    STAMP(8);
    v4u hf[7];
    unsigned hb[4];
#pragma unroll
    for (int pt = 0; pt < 4; ++pt) hb[pt] = WS_HT + ((rt * 4 + ((pt + ct) & 3)) * 54 + w) * 1024;
#pragma unroll
    for (int i = 0; i < 7; ++i) hf[i] = ld16(rws, w2v, hb[0] + (i < 6 ? i * 8192u : k6));
    PIN();
    if (w < 4 && spare) {
      WAITV(7);
      if (lds_arrive(S, 10, wq) && lane == 0) put_flag(ws, FL_Y + yi, tag);
    }
    bar();
    STAMP(9);
    LAS char* Pw = S + w * GW + g * 256 + (((lane & 15) + g) & 15) * 16;
    const __amdgpu_buffer_rsrc_t rx = __builtin_amdgcn_make_buffer_rsrc((void*)x, 0, L * D * 2, 0x00020000),
                                 ro = __builtin_amdgcn_make_buffer_rsrc(out, 0, L * D * 2, 0x00020000),
                                 rb2 = __builtin_amdgcn_make_buffer_rsrc((void*)b2, 0, D * 2, 0x00020000);
    const int fyk = FL_Y + ct * 32 + (lane & 31);
    unsigned tpr[3], tov[3], tyr[3];
    v2u xr[3], bb[3], yb0;
    v4u yv[2];
    auto task_setup = [&](int i) {
      const int id = i == 0 ? w : i == 1 ? w + 8 : w + 15, pt = id / 5, idx = (id % 5) * 64 + lane, et = idx / 20, eq = idx % 20, t = ((pt + ct) & 3) * 16 + et;
      const bool on = i == 0 || (i == 1 ? w < 7 : w < 5);
      tpr[i] = (pt >= 3 ? 0 : pt) * GBUF + (eq >> 2) * GT + (eq & 3) * 256 + ((et + (eq & 3)) & 15) * 16;
      tov[i] = ((rt * 64 + t) * D + ct * 80 + 4 * eq) * 2 + (on ? 0u : OOB);
      tyr[i] = YT2 + t * YT2ROW + 8 * eq;
      xr[i] = __builtin_amdgcn_raw_buffer_load_b64(rx, tov[i], 0, 0);
      bb[i] = __builtin_amdgcn_raw_buffer_load_b64(rb2, (ct * 80 + 4 * eq) * 2, 0, 0);
    };
    auto reduce = [&](int i) {
      f4 pr[8];
#pragma unroll
      for (int v = 0; v < 8; ++v) pr[v] = *(LAS f4*)(S + tpr[i] + v * GW);
      const f4 sa = ((pr[0] + pr[1]) + (pr[2] + pr[3])) + ((pr[4] + pr[5]) + (pr[6] + pr[7]));
      return v2u{pk2(unpk(pk2(sa.xy)) + unpk(bb[i].x)), pk2(unpk(pk2(sa.zw)) + unpk(bb[i].y))};
    };
    auto finish = [&](int i, v2u yb) {
      const v2u y = *(LAS v2u*)(S + tyr[i]);
      __builtin_amdgcn_raw_buffer_store_b64(v2u{pk2(unpk(pk2(unpk(xr[i].x) + unpk(y.x))) + unpk(yb.x)), pk2(unpk(pk2(unpk(xr[i].y) + unpk(y.y))) + unpk(yb.y))}, ro, tov[i], 0, 0);
    };
    f4 acc[2][5];
#pragma unroll
    for (int pt = 0; pt < 4; ++pt) {
#pragma unroll
      for (int j = 0; j < 7; ++j) {
        if (pt == 3 && j >= 5) {
          const int c = lane + 64 * (j - 5);
          LAS u16* d = (LAS u16*)(S + YT2 + (c & 7) * 8 * YT2ROW) + 10 * w + (c >> 3);
          if (c < 80) {
#pragma unroll
            for (int e = 0; e < 4; ++e) { d[(2 * e) * (YT2ROW / 2)] = (u16)yv[j - 5][e]; d[(2 * e + 1) * (YT2ROW / 2)] = (u16)(yv[j - 5][e] >> 16); }
          }
        }
        if (j >= 1 && j < 6 && pt >= 1) *(LAS f4*)(Pw + (pt - 1) * GBUF + (j - 1) * GT) = acc[(pt - 1) & 1][j - 1];
#pragma unroll
        for (int n = 0; n < 5; ++n)
          acc[pt & 1][n] = __builtin_amdgcn_mfma_f32_16x16x32_bf16(asbf(wf2[5 * j + n]), asbf(hf[j]), j ? acc[pt & 1][n] : f4{0.f, 0.f, 0.f, 0.f}, 0, 0, 0);
        if (pt < 3) hf[j] = ld16(rws, w2v, hb[pt + 1] + (j < 6 ? j * 8192u : k6));
        PIN();
        if (pt == 0 && j == 2 && w < 4 && !spare) {
          WAITV(7);
          if (lds_arrive(S, 10, wq) && lane == 0) put_flag(ws, FL_Y + yi, tag);
        }
        if (pt == 1 && j == 3) fh = ld_hidden(ws, WS_FLAG + 128u * fyk);
        if (pt == 2 && j == 3) {
          WAITV(7);
          GOT(fh);
          check_flags(ws, fyk, fh, tag);
#pragma unroll
          for (int k = 0; k < 2; ++k) {
            const int c = lane + 64 * k;
            yv[k] = ld16(rws, (10 * w + (c >> 3)) * 4096 + (c & 7) * 16 + (c < 80 ? 0u : OOB), WS_YT + ct * 80 * 4096 + rt * 128);
          }
          PIN();
        }
      }
      if (pt == 0) STAMP(11);
      if (pt == 1) STAMP(14);
      if (pt == 2) { task_setup(0); task_setup(1); task_setup(2); PIN(); }
    }
    if (w < 4) STAMP(12);
    bar();
    yb0 = reduce(0);
    GOT(yb0);
    bar();
#pragma unroll
    for (int n = 0; n < 5; ++n) *(LAS f4*)(Pw + n * GT) = acc[1][n];
    finish(0, yb0);
    finish(1, reduce(1));
    bar();
    if (w < 4) STAMP(13);
    finish(2, reduce(2));
  }
  STAMP(10);
  STAMP_OUT();
  if (b == 0 && w == 7) {
#pragma unroll 1
    for (unsigned s = 0, j = 0; s < SPIN_CAP && j < 4; ++s)
      if (__all((unsigned)(ld_word(ws, WS_ST + (64 * j + lane) * 8) >> 32) == (unsigned)tag)) ++j;
    if (lane == 0) __hip_atomic_store((GAS u64*)ws, tag, __ATOMIC_RELAXED, __HIP_MEMORY_SCOPE_AGENT);
  }
  asm volatile("s4_code_end:");
}
}

extern "C" void kernel_launch(void* const* d_in, const int* in_sizes, int n_in, void* d_out, int out_size, void* d_ws, size_t ws_size,
                              hipStream_t stream) {
  (void)in_sizes; (void)n_in; (void)out_size; (void)ws_size;
  (void)hipFuncSetAttribute((const void*)s4mlp_kernel, hipFuncAttributeMaxDynamicSharedMemorySize, LDS_BYTES);
  s4mlp_kernel<<<256, 512, LDS_BYTES, stream>>>((const u16*)d_in[0], (const u16*)d_in[1], (const u16*)d_in[2], (const u16*)d_in[3], (const u16*)d_in[4],
                                                (const u16*)d_in[5], (const u16*)d_in[6], (const u16*)d_in[11], (const u16*)d_in[7], (const u16*)d_in[8],
                                                (const u16*)d_in[10], (const u16*)d_in[12], (const u16*)d_in[13], (u16*)d_out, (char*)d_ws);
}
```

```cpp
#include <hip/hip_runtime.h>
#include <hip/hip_bf16.h>
#include <stdint.h>

namespace {
typedef unsigned short u16;
typedef unsigned long long u64;
typedef __bf16 bf8 __attribute__((ext_vector_type(8)));
typedef __bf16 bf2 __attribute__((ext_vector_type(2)));
typedef short s4 __attribute__((ext_vector_type(4)));
typedef float f16v __attribute__((ext_vector_type(16)));
typedef float f4 __attribute__((ext_vector_type(4)));
typedef float f2 __attribute__((ext_vector_type(2)));
typedef unsigned v4u __attribute__((ext_vector_type(4)));
typedef unsigned v2u __attribute__((ext_vector_type(2)));
#define LAS __attribute__((address_space(3)))
#define GAS __attribute__((address_space(1)))
#define DI __device__ __forceinline__

constexpr int L = 2048, D = 640, N = 128, F = 1728;
constexpr unsigned WS_END0 = (1u << 20) + 168u * 54 * 1024 + 2 * 640u * 4096;
constexpr unsigned WS_FLAG = 4096;
constexpr int FL_H = 0, FL_X = 224, FL_Y = 384, FL_K = 608, FL_W = 640;
constexpr unsigned WS_ST = 256u << 10;
constexpr unsigned WS_CS = WS_ST + 2048 * 8;
constexpr unsigned WS_K3 = WS_END0;
constexpr unsigned WS_HT = 1u << 20;
constexpr unsigned WS_W2T = WS_HT + 128u * 54 * 1024;
constexpr unsigned WS_XT = WS_W2T + 40u * 54 * 1024;
constexpr unsigned WS_YT = WS_XT + 640u * 4096;
constexpr unsigned WS_END = WS_YT + 2 * 640u * 4096;
static_assert(WS_K3 == WS_YT + 640u * 4096, "workspace map");
constexpr unsigned WS_DBG = 32u << 20;
constexpr unsigned OOB = 0x40000000u;
constexpr int LDS_BYTES = 163840;
constexpr int SLOT = 32768;
constexpr int TAB = 98304, TROW = 272, TW = 32 * TROW;
constexpr int CNT = TAB + 4 * TW, TAGL = CNT + 240;
constexpr int COLT = CNT + 256;
constexpr int W2U_G = COLT + 2048, W2U_S = 16384;
constexpr int KCH = 4608, KARR = LDS_BYTES - 6 * KCH;
constexpr int UCP = 65536, UST = 4160, UCH = 4 * UST;
constexpr int GT = 1088, GW = 5 * GT, GBUF = 8 * GW, YT2 = CNT + 256, YT2ROW = 168;
static_assert(W2U_G + 4096 <= KARR + 3 * KCH && UCP + 3 * UCH <= CNT && 3 * GBUF <= CNT && YT2 + 64 * YT2ROW <= LDS_BYTES, "LDS map");
#define NB1 20
#define NB0 8
#define WARM 0
#define NA 0
#define LAUX 16
constexpr unsigned SPIN_CAP = 1u << 17;
#define SLP 1

DI float bflo(unsigned v) { return __uint_as_float(v << 16); }
DI float bfhi(unsigned v) { return __uint_as_float(v & 0xffff0000u); }
DI f2 unpk(unsigned v) { return f2{bflo(v), bfhi(v)}; }
DI unsigned pk2(f2 p) { return __builtin_bit_cast(unsigned, __builtin_convertvector(p, bf2)); }
DI unsigned pk(float a, float b) { return pk2(f2{a, b}); }
DI float rbf(float a) { return bflo(pk(a, a)); }
DI bf8 asbf(v4u v) { return __builtin_bit_cast(bf8, v); }
DI v4u mk_srd(const void* p, unsigned bytes) { u64 a = (u64)p; return v4u{(unsigned)a, (unsigned)(a >> 32) & 0xffffu, bytes, 0x00020000u}; }
#define SET_M0(v) asm volatile("s_mov_b32 m0, %0\n\ts_nop 0" ::"s"(__builtin_amdgcn_readfirstlane(v)) : "memory")
#define DMA(srd, voff, soff, imm) asm volatile("buffer_load_dwordx4 %0, %1, %2 offen offset:" #imm " lds" ::"v"(voff), "s"(srd), "s"(soff) : "memory")
#define WAITV(n) asm volatile("s_waitcnt vmcnt(" #n ")" ::: "memory")
#define PIN() __builtin_amdgcn_sched_barrier(0)
#define ORDER() asm volatile("" ::: "memory")
DI void bar() { asm volatile("s_waitcnt lgkmcnt(0)\n\ts_barrier" ::: "memory"); }
DI void bar_raw() { PIN(); asm volatile("s_barrier" ::: "memory"); PIN(); }
DI v2u trd(LAS char* p) { return __builtin_bit_cast(v2u, __builtin_amdgcn_ds_read_tr16_b64_v4i16((LAS s4*)p)); }
DI v4u trd2(LAS char* p0, LAS char* p1) { v2u a = trd(p0), b = trd(p1); return v4u{a.x, a.y, b.x, b.y}; }
DI float dot2(unsigned a, unsigned b, float c) { return __builtin_amdgcn_fdot2_f32_bf16(__builtin_bit_cast(bf2, a), __builtin_bit_cast(bf2, b), c, false); }
DI float dot8(v4u a, v4u b, float c) { return dot2(a.w, b.w, dot2(a.z, b.z, dot2(a.y, b.y, dot2(a.x, b.x, c)))); }
DI float sum8(v4u a, float c) { const unsigned o = 0x3f803f80u; return dot2(a.w, o, dot2(a.z, o, dot2(a.y, o, dot2(a.x, o, c)))); }
DI f2 fma2(f2 a, f2 b, f2 c) { return __builtin_elementwise_fma(a, b, c); }
struct cm { f2 p, n; };
DI cm mkm(f2 b) { return cm{b, f2{-b.y, b.x}}; }
DI f2 cmul(f2 a, cm b) { return fma2(a.yy, b.n, a.xx * b.p); }
DI f2 cexpl(float a, float th, float l) {
  const float m = __builtin_amdgcn_exp2f(a * l), rv = __builtin_amdgcn_fractf(th * l);
  return f2{m, m} * f2{__builtin_amdgcn_cosf(rv), __builtin_amdgcn_sinf(rv)};
}
DI float xor32(float v) { const auto r = __builtin_amdgcn_permlane32_swap(__float_as_uint(v), __float_as_uint(v), false, false); return __uint_as_float(r[0]) + __uint_as_float(r[1]); }
DI void st16(__amdgpu_buffer_rsrc_t r, v4u v, unsigned off) { __builtin_amdgcn_raw_buffer_store_b128(v, r, off, 0, 16); }
DI v4u ld16(__amdgpu_buffer_rsrc_t r, unsigned voff, unsigned soff) { return __builtin_amdgcn_raw_buffer_load_b128(r, voff, soff, LAUX); }
DI void put_flag(char* ws, int k, u64 tag) { __hip_atomic_store((GAS u64*)(ws + WS_FLAG + 128u * k), tag, __ATOMIC_RELAXED, __HIP_MEMORY_SCOPE_AGENT); }
DI u64 ld_word(char* ws, unsigned off) { return __hip_atomic_load((GAS u64*)(ws + off), __ATOMIC_RELAXED, __HIP_MEMORY_SCOPE_AGENT); }
DI void wait_flags(char* ws, int k, u64 tag) {
#pragma unroll 1
  for (unsigned s = 0; s < SPIN_CAP; ++s) {
    if (__all(ld_word(ws, WS_FLAG + 128u * k) == tag)) break;
    __builtin_amdgcn_s_sleep(1);
  }
  __builtin_amdgcn_fence(__ATOMIC_ACQUIRE, "wavefront");
}
DI u64 next_tag(u64 ep) { return ep * 6364136223846793005ull + 1442695040888963407ull; }
DI float ld_bf_uniform(const u16* p, int i) { const unsigned v = ((const unsigned*)p)[i >> 1]; return (i & 1) ? bfhi(v) : bflo(v); }
DI void lds_put(unsigned addr, unsigned v) { asm volatile("s_mov_b64 exec, 1\n\tds_write_b32 %0, %1\n\ts_mov_b64 exec, -1" ::"v"(addr), "v"(v) : "memory"); }
DI bool lds_arrive(LAS char* S, int c, int q) {
  asm volatile("s_waitcnt lgkmcnt(0)" ::: "memory");
  lds_put(CNT + 16 * c + 4 * q, 1u);
  const v4u v = *(volatile LAS v4u*)(S + CNT + 16 * c);
  return __builtin_amdgcn_readfirstlane(v.x & v.y & v.z & v.w);
}
DI void lds_wait(LAS char* S, int c) {
#pragma unroll 1
  for (unsigned s = 0; s < (SPIN_CAP << 4); ++s) {
    const v4u v = *(volatile LAS v4u*)(S + CNT + 16 * c);
    if (__builtin_amdgcn_readfirstlane(v.x & v.y & v.z & v.w)) break;
    __builtin_amdgcn_s_sleep(SLP);
  }
  ORDER();
}
DI void lds_set(LAS char* S, int off, unsigned v) { asm volatile("s_waitcnt lgkmcnt(0)" ::: "memory"); lds_put(off, v); }
DI unsigned lds_get(LAS char* S, unsigned off) { return *(volatile LAS unsigned*)(S + off); }
DI void lds_wait_ge(LAS char* S, unsigned off, unsigned n) {
#pragma unroll 1
  for (unsigned s = 0; s < (SPIN_CAP << 4); ++s) {
    if (__all(lds_get(S, off) >= n)) break;
    __builtin_amdgcn_s_sleep(SLP);
  }
  ORDER();
}
DI u64 rd_tag(LAS char* S) { const v2u t = *(volatile LAS v2u*)(S + TAGL); return ((u64)(unsigned)__builtin_amdgcn_readfirstlane(t.y) << 32) | (unsigned)__builtin_amdgcn_readfirstlane(t.x); }
DI u64 get_tag(LAS char* S) { lds_wait_ge(S, TAGL + 8, 1); return rd_tag(S); }
DI u64 ld_hidden(char* ws, unsigned off) { u64 v; asm volatile("global_load_dwordx2 %0, %1, %2 sc1" : "=v"(v) : "v"(off), "s"(ws) : "memory"); return v; }
DI float sadd(float a, float b) { float r; asm("v_add_f32 %0, %1, %2" : "=v"(r) : "v"(a), "v"(b)); return r; }
DI float smul(float a, float b) { float r; asm("v_mul_f32 %0, %1, %2" : "=v"(r) : "v"(a), "v"(b)); return r; }
DI float ssub(float a, float b) { float r; asm("v_sub_f32 %0, %1, %2" : "=v"(r) : "v"(a), "v"(b)); return r; }
DI bool tag_ok(u64 g, u64 tag) { return __all((unsigned)(g >> 32) == (unsigned)tag); }
DI u64 wait_granule(char* ws, unsigned off, u64 tag) {
  u64 g = 0;
#pragma unroll 1
  for (unsigned s = 0; s < SPIN_CAP; ++s) { g = ld_word(ws, off); if (tag_ok(g, tag)) break; }
  return g;
}
#define GOT(v) asm volatile("" : "+v"(v))
DI void check_flags(char* ws, int k, u64 seen, u64 tag) {
  if (!__all(seen == tag)) wait_flags(ws, k, tag);
  __builtin_amdgcn_fence(__ATOMIC_ACQUIRE, "wavefront");
}
#define STAMP(k)
#define STAMP_DECL
#define STAMP_OUT()

__global__ __launch_bounds__(512, 2) void s4mlp_kernel(const u16* __restrict__ x, const u16* __restrict__ Are, const u16* __restrict__ Aim,
                                                      const u16* __restrict__ Bre, const u16* __restrict__ Bim, const u16* __restrict__ Cre,
                                                      const u16* __restrict__ Cim, const u16* __restrict__ b1, const u16* __restrict__ logdt,
                                                      const u16* __restrict__ dskip, const u16* __restrict__ w1, const u16* __restrict__ w2,
                                                      const u16* __restrict__ b2, u16* __restrict__ out, char* ws) {
  LAS char* const S = (LAS char*)0;
  const int tid = threadIdx.x, lane = tid & 63, w = __builtin_amdgcn_readfirstlane(tid >> 6), wq = w & 3;
  const int b = blockIdx.x;
  STAMP_DECL;
  STAMP(0);
  v4u pf0 = {}, pf1 = {};
  if (w == 7) {
    unsigned len;
    asm volatile("s4_code_begin:\n\ts_mov_b32 %0, s4_code_end-s4_code_begin-256" : "=s"(len));
    const v4u sc = mk_srd((const void*)__builtin_amdgcn_s_getpc(), __builtin_amdgcn_readfirstlane(len));
    asm volatile("s_nop 4\n\tbuffer_load_dwordx4 %0, %2, %3, 0 offen\n\tbuffer_load_dwordx4 %1, %4, %3, 0 offen"
                 : "=&v"(pf0), "=&v"(pf1) : "v"((b >> 3) * 1024 + lane * 16), "s"(sc), "v"((b >> 3) * 1024 + lane * 16 + 32768) : "memory");
    *(LAS unsigned*)(S + CNT + 4 * lane) = 0u;
  }
  asm volatile("" ::"s"(x), "s"(w1), "s"(ws), "s"(Are), "s"(Aim), "s"(Bre), "s"(Bim), "s"(Cre), "s"(Cim), "s"(w2), "s"(logdt), "s"(dskip));
  bar();
#define IDS()                                                                                                              \
  int bl = b;                                                                                                              \
  asm volatile("" : "+s"(bl));                                                                                             \
  bl = __builtin_amdgcn_readfirstlane(bl);                                                                                 \
  const int xcd = bl & 7, bi = bl >> 3;                                                                                    \
  const int rp = 4 * (xcd >> 1) + (bi >> 3), cb = 7 * (xcd & 1) + (bi & 7);                                 \
  const bool spare = (bi & 7) == 7;                                                                       \
  const int c0 = cb * 128 > F - 128 ? F - 128 : cb * 128, gi = rp * 14 + cb, sp = xcd * 4 + (bi >> 3);                      \
  const int gq = gi / 28, gm = gi % 28;   \
  const int ch00 = spare ? 80 * (sp >> 2) + 56 + 6 * (sp & 3) : 80 * gq + 2 * gm, ch3 = 80 * gq + 56 + gm;                   \
  const int nbat = spare ? 3 : 1, nch = gm < 24 ? 3 : 2;                                                                   \
  const int rt = 8 * (xcd >> 1) + (bi >> 2), ct = 4 * (xcd & 1) + (bi & 3);                                 \
  const int kar = spare ? KARR : KARR + 3 * KCH;                                                                             \
  const int s0 = (cb * 3 + rp * 5 + (rp >> 1)) % 10;             \
  const unsigned w2s = WS_W2T + (ct * 270 + w) * 1024;                                                                      \
  const int fhk = FL_H + (rt >> 1) * 14 + (lane < 14 ? lane : 0);                                                          \
  (void)c0; (void)ch3; (void)nch; (void)ch00; (void)nbat; (void)kar; (void)s0; (void)w2s; (void)fhk
  const __amdgpu_buffer_rsrc_t rws = __builtin_amdgcn_make_buffer_rsrc(ws, 0, WS_END, 0x00020000);
  const int h = lane >> 5, g = lane >> 4, q = (lane & 15) >> 2, p = lane & 3, blk = g & 1;
  auto fw_flag = [&](int ct) { const int k = (lane >> 1) * 8 + w; return FL_W + (k < 54 ? k : w) * 10 + ((ct * 80 + (lane & 1) * 79) >> 6); };
  const unsigned k6 = w < 6 ? 49152u : 0u;
  v4u wf2[35];
  const unsigned w2v = lane * 16, w2v6 = w2v + (w < 6 ? 0u : OOB);
  auto load_w2_ = [&](unsigned w2s, int i0, int i1) {
#pragma unroll
    for (int i = 0; i < 35; ++i)
      if (i >= i0 && i < i1) wf2[i] = ld16(rws, i >= 30 ? w2v6 : w2v, w2s + (i % 5) * (54 * 1024) + (i / 5) * 8192);
  };
  auto warm_w2 = [&](int c2, u64 tag) {
    wait_flags(ws, fw_flag(c2), tag);
    load_w2_(WS_W2T + (c2 * 270 + w) * 1024, 0, 35);
#pragma unroll
    for (int i = 0; i < 35; ++i) GOT(wf2[i]);
  };
  u64 tag, fh = 0;
  if (w >= 4) {
    IDS();
    int sn = s0;
    const int u0 = spare ? 448 + 3 * sp : 2 * gi;
    const v4u sx = mk_srd(x, L * D * 2), sw1 = mk_srd(w1, D * F * 2);
    unsigned vx[4], vw[4];
    auto ring_setup = [&]() {
      int ln = lane;
      asm volatile("" : "+v"(ln));
#pragma unroll
      for (int j = 0; j < 4; ++j) {
        const int rx = 8 * j + (ln >> 3);
        vx[j] = (rp * 128 + rx) * (D * 2) + 16 * ((ln & 7) ^ ((rx >> 1) & 7)) - 1024 * j;
        vw[j] = (4 * j + (ln >> 4)) * (F * 2) + c0 * 2 + 16 * ((ln & 15) ^ (((ln >> 4) << 2) | j)) - 1024 * j;
      }
    };
    auto quarter = [&](int st, int slot, int k) {
      const unsigned so = st * 128 + k * (32 * D * 2), sv = st * (64 * F * 2) + k * (16 * F * 2);
      SET_M0(slot * SLOT + k * 4096);
      DMA(sx, vx[0], so, 0); DMA(sx, vx[1], so, 1024); DMA(sx, vx[2], so, 2048); DMA(sx, vx[3], so, 3072);
      SET_M0(slot * SLOT + 16384 + k * 4096);
      DMA(sw1, vw[0], sv, 0); DMA(sw1, vw[1], sv, 1024); DMA(sw1, vw[2], sv, 2048); DMA(sw1, vw[3], sv, 3072);
    };
    if (w == 7) {
      const __amdgpu_buffer_rsrc_t rx = __builtin_amdgcn_make_buffer_rsrc((void*)x, 0, L * D * 2, 0x00020000),
                                   rw2 = __builtin_amdgcn_make_buffer_rsrc((void*)w2, 0, D * F * 2, 0x00020000);
      const u64 ep = ld_word(ws, 0);
      v4u xv[10], wv[3][4];
#pragma unroll
      for (int i = 0; i < 10; ++i) xv[i] = __builtin_amdgcn_raw_buffer_load_b128(rx, (lane >> 3) * (D * 2) + (lane & 7) * 16 + i * 128, b * (8 * D * 2), 0);
#pragma unroll
      for (int k = 0; k < 3; ++k) {
        const int u = u0 + k;
        const unsigned vo = (lane >> 3) * (D * 2) + (lane & 7) * 16 + ((k < 2 || spare) && u < 540 ? 0u : OOB);
#pragma unroll
        for (int i = 0; i < 4; ++i) wv[k][i] = __builtin_amdgcn_raw_buffer_load_b128(rw2, vo, ((u / 10) * 32 + 8 * i) * (D * 2) + (u % 10) * 128, 0);
      }
      PIN();
      ring_setup();
      const u64 tg = next_tag(ep);
      lds_put(TAGL, (unsigned)tg); lds_put(TAGL + 4, (unsigned)(tg >> 32));
      lds_set(S, TAGL + 8, 1);
      GOT(pf0); GOT(pf1);
      float sm[2] = {}, sq[2] = {};
#pragma unroll
      for (int i = 0; i < 10; ++i) { sm[i & 1] = sum8(xv[i], sm[i & 1]); sq[i & 1] = dot8(xv[i], xv[i], sq[i & 1]); }
      float sum = sm[0] + sm[1], ssq = sq[0] + sq[1];
#pragma unroll
      for (int m = 1; m < 8; m <<= 1) { sum += __shfl_xor(sum, m); ssq += __shfl_xor(ssq, m); }
      const float mean = sum * (1.f / D), var = fmaxf(ssq * (1.f / D) - mean * mean, 0.f);
      const unsigned ms = pk(mean, __builtin_amdgcn_sqrtf(rbf(rbf(var) + 1e-5f)));
      if ((lane & 7) == 0) __hip_atomic_store((GAS u64*)(ws + WS_ST + (lane >> 3) * 2048 + b * 8), (tg << 32) | ms, __ATOMIC_RELAXED, __HIP_MEMORY_SCOPE_AGENT);
      STAMP(1);
      LAS char* U = S + (spare ? W2U_S : W2U_G);
      LAS char* B = U + (4 * (blk + 4 * (g >> 1)) + q) * 128 + 8 * p;
      GOT(wv[2][0]); GOT(wv[2][1]); GOT(wv[2][2]); GOT(wv[2][3]);
#pragma unroll
      for (int k = 0; k < 3; ++k) {
        const int u = u0 + k;
        if ((k < 2 || spare) && u < 540) {
#pragma unroll
          for (int i = 0; i < 4; ++i) *(LAS v4u*)(U + i * 1024 + lane * 16) = wv[k][i];
#pragma unroll
          for (int n = 0; n < 4; ++n) st16(rws, trd2(B + 32 * n, B + 32 * n + 1024), lane * 16 + WS_W2T + (((u % 10) * 4 + n) * 54 + u / 10) * 1024);
        }
      }
    } else if (!spare) {
      ring_setup();
#pragma unroll 1
      for (int t = wq; t < 12; t += 3) {
        if (t >= 9) { WAITV(8); lds_put(CNT + 28, 1); lds_put(CNT + 16 + 4 * wq, 1); }
        quarter(sn + (t >> 2) >= 10 ? sn + (t >> 2) - 10 : sn + (t >> 2), t >> 2, t & 3);
      }
      WAITV(8);
      STAMP(1);
    } else if (sp < 27) {
      const __amdgpu_buffer_rsrc_t rw1 = __builtin_amdgcn_make_buffer_rsrc((void*)w1, 0, D * F * 2, 0x00020000);
      v4u v[27];
#pragma unroll
      for (int i = 0; i < 27; ++i) v[i] = __builtin_amdgcn_raw_buffer_load_b128(rw1, (216 * wq + 8 * i + (lane >> 3)) * (F * 2) + (lane & 7) * 16, sp * 128, 0);
      PIN();
      f2 a[4] = {};
#pragma unroll
      for (int i = 0; i < 27; ++i)
#pragma unroll
        for (int e = 0; e < 4; ++e) a[e] += unpk(v[i][e]);
      *(LAS f4*)(S + (wq * 64 + lane) * 32) = f4{a[0].x, a[0].y, a[1].x, a[1].y};
      *(LAS f4*)(S + (wq * 64 + lane) * 32 + 16) = f4{a[2].x, a[2].y, a[3].x, a[3].y};
      lds_put(CNT + 16 + 12, 1);
      if (lds_arrive(S, 1, wq)) {
        float t = 0.f;
#pragma unroll
        for (int i = 0; i < 24; ++i) t += *(LAS float*)(S + i * 256 + lane * 4);
        __hip_atomic_store((GAS u64*)(ws + WS_CS + (sp * 64 + lane) * 8), (get_tag(S) << 32) | __float_as_uint(t), __ATOMIC_RELAXED, __HIP_MEMORY_SCOPE_AGENT);
      }
    }
    int fwk = fw_flag(ct);
    GOT(fwk);
    STAMP(2);
    if (spare) {
      tag = get_tag(S);
      if (w == 7) {
        WAITV(0);
        if (lane < 3 && u0 + lane < 540) put_flag(ws, FL_W + u0 + lane, tag);
      }
      if ((bi >> 3) != 3) warm_w2(4 * (xcd & 1) + (bi >> 3), tag);
      wait_flags(ws, fwk, tag);
      load_w2_(w2s, 0, NB1);
    } else {
      const int t4 = tid - 256;
      const int fxk = FL_X + (lane & 15) * 10 + ((g == 2 && nch == 3 ? ch3 : ch00 + (g == 1)) >> 6);
      bar();
      STAMP(6);
      tag = rd_tag(S);
      sn = sn >= 7 ? sn - 7 : sn + 3;
#pragma unroll 1
      for (int n = 0; n < 6; ++n) {
        quarter(sn, (n + 3) & 3, wq);
        sn = sn == 9 ? 0 : sn + 1;
        WAITV(8);
        if (n == 0 && w == 7) { if (lane < 2) put_flag(ws, FL_W + u0 + lane, tag); }
        bar_raw();
      }
      quarter(sn, 1, wq);
      u64 pw = ld_hidden(ws, WS_FLAG + 128u * fwk), px = ld_hidden(ws, WS_FLAG + 128u * fxk);
      v2u gr[8];
#pragma unroll
      for (int i = 0; i < 8; ++i) gr[i] = __builtin_amdgcn_raw_buffer_load_b64(rws, t4 * 8, WS_ST + i * 2048, 16);
      WAITV(18);
      bar_raw();
      STAMP(4);
      WAITV(8);
      bar_raw();
      STAMP(5);
      GOT(pw); GOT(px);
      check_flags(ws, fxk, px, tag);
      v4u un[3];
#pragma unroll
      for (int j = 0; j < 3; ++j) un[j] = ld16(rws, t4 * 16 + (j < nch ? 0u : OOB), WS_XT + (j < 2 ? ch00 + j : ch3) * 4096);
      ORDER();
      check_flags(ws, fwk, pw, tag);
      load_w2_(w2s, 0, NB0);
      PIN();
      STAMP(1);
      __builtin_amdgcn_s_setprio(1);
      {
        unsigned bad = 0;
#pragma unroll
        for (int i = 0; i < 8; ++i) bad |= gr[i].y ^ (unsigned)tag;
        if (!__all(bad == 0)) {
#pragma unroll
          for (int i = 0; i < 8; ++i) gr[i].x = (unsigned)wait_granule(ws, WS_ST + i * 2048 + t4 * 8, tag);
        }
        float rr[8];
#pragma unroll
        for (int e = 0; e < 8; ++e) rr[e] = __builtin_amdgcn_rcpf(bfhi(gr[e].x));
        asm volatile("s_nop 0" : "+v"(rr[0]), "+v"(rr[1]), "+v"(rr[2]), "+v"(rr[3]), "+v"(rr[4]), "+v"(rr[5]), "+v"(rr[6]), "+v"(rr[7]));
#pragma unroll
        for (int e = 0; e < 4; ++e) {
          const float n0 = bflo(gr[2 * e].x), n1 = bflo(gr[2 * e + 1].x);
#pragma unroll
          for (int j = 0; j < 3; ++j) un[j][e] = pk(smul(ssub(bflo(un[j][e]), n0), rr[2 * e]), smul(ssub(bfhi(un[j][e]), n1), rr[2 * e + 1]));
        }
        LAS char* U = S + UCP + 32 + t4 * 16;
        if (t4 < 56) *(LAS v4u*)(S + (t4 < 24 ? UCP + (t4 >> 1) * UST + (t4 & 1) * 16 : KARR + 5 * KCH + (t4 - 24) * 16)) = v4u{0u, 0u, 0u, 0u};
#pragma unroll
        for (int j = 0; j < 3; ++j) *(LAS v4u*)(U + j * UCH) = un[j];
        lds_put(CNT + 16 * 7 + 4 * wq, 1);
        lds_wait(S, 7);
#pragma unroll
        for (int j = 0; j < 3; ++j) {
          const v4u d = un[j]; const v2u pv = *(LAS v2u*)(U + j * UCH - 8);
          const unsigned a0 = __builtin_amdgcn_alignbit(pv.y, pv.x, 16), a1 = __builtin_amdgcn_alignbit(d.x, pv.y, 16), a2 = __builtin_amdgcn_alignbit(d.y, d.x, 16),
                         a3 = __builtin_amdgcn_alignbit(d.z, d.y, 16), a4 = __builtin_amdgcn_alignbit(d.w, d.z, 16);
          *(LAS v4u*)(U + j * UCH + UST) = v4u{a1, a2, a3, a4};
          *(LAS v4u*)(U + j * UCH + 2 * UST) = v4u{pv.y, d.x, d.y, d.z};
          *(LAS v4u*)(U + j * UCH + 3 * UST) = v4u{a0, a1, a2, a3};
        }
      }
      if (nch == 3) wait_flags(ws, FL_K + 4 * gq + gm / 6, tag);
      const v4u k3 = ld16(rws, t4 * 16, WS_K3 + ch3 * 4096);
      ORDER();
      load_w2_(w2s, NB0, NB1);
      *(LAS v4u*)(S + KARR + 5 * KCH + 512 + t4 * 16) = k3;
      lds_put(CNT + 16 * 8 + 4 * wq, 1);
      STAMP(12);
      LAS char* Kp = S + kar + 512 + 32 * ((lane & 15) - (g >> 1)) + 16 * blk - 64 * wq - 256;
      LAS char* Up = S + UCP + q * UST + 32 + 32 * (g >> 1) - 16 * blk + 8 * p + 64 * wq - 8;
      LAS char* const Pp0 = S + wq * 8192 + g * 256 + (((lane & 15) ^ (2 * g)) * 16);
      lds_wait(S, 8);
      v4u kf[8], kg[8], ua[2], ub[2];
      auto rd_u = [&](int o, int s) {
        ua[s & 1] = trd2(Up + o * UCH + 512 * s + 8, Up + o * UCH + 512 * s); ub[s & 1] = trd2(Up + o * UCH + 512 * (7 - s) + 264, Up + o * UCH + 512 * (7 - s) + 256);
        kg[s] = *(LAS v4u*)(Kp + o * KCH + 512 * s);
      };
      rd_u(0, 0);
#pragma unroll
      for (int e = 7; e >= 0; --e) kf[e] = *(LAS v4u*)(Kp + 512 * e + 256);
      lds_wait(S, 6);
      STAMP(13);
#pragma unroll 1
      for (int cj = 0; cj < nch; ++cj) {
        f4 acc[8];
        PIN();
#pragma unroll
        for (int s = 0; s < 8; ++s) {
          if (s < 7) rd_u(0, s + 1);
          if (s > 0) kf[8 - s] = *(LAS v4u*)(Kp + KCH + 512 * (8 - s) + 256);
#pragma unroll
          for (int R = 7; R >= s; --R) acc[R] = __builtin_amdgcn_mfma_f32_16x16x32_bf16(asbf(ua[s & 1]), asbf(kf[R - s]), s ? acc[R] : f4{0.f, 0.f, 0.f, 0.f}, 0, 0, 0);
#pragma unroll
          for (int R = 7 - s; R < 8; ++R) acc[R] = __builtin_amdgcn_mfma_f32_16x16x32_bf16(asbf(ub[s & 1]), asbf(kg[R - 7 + s]), acc[R], 0, 0, 0);
#pragma unroll
          for (int i = 0; i < 6; ++i) { __builtin_amdgcn_sched_group_barrier(0x8, 1, 0); __builtin_amdgcn_sched_group_barrier(0x80, 1, 0); }
          PIN();
        }
        kf[0] = *(LAS v4u*)(Kp + KCH + 256);
        rd_u(1, 0);
        if (cj == 2) lds_wait(S, 9);
        PIN();
#pragma unroll
        for (int R = 0; R < 8; ++R) *(LAS f4*)(Pp0 + (cj & 1) * SLOT + R * 1024) = acc[R];
        lds_put(CNT + 16 * 2 + 4 * wq, cj + 1);
        Kp += KCH; Up += UCH;
      }
    }
    STAMP(7);
    __builtin_amdgcn_s_setprio(0);
    load_w2_(w2s, NB1, 35);
  } else {
    IDS();
    const int wf = w >> 1, tg = w & 1;
    unsigned xa[4], wa[2][2], pn[6];
    unsigned lgn, dskn;
    auto fetch = [&](int bt) {
      const int ch = ch00 + 2 * bt + wf, o = ch * N + tg * 64 + lane;
      pn[0] = Are[o]; pn[1] = Aim[o]; pn[2] = Bre[o]; pn[3] = Bim[o]; pn[4] = Cre[o]; pn[5] = Cim[o];
      lgn = logdt[ch]; dskn = dskip[ch];
      PIN();
    };
    fetch(0);
    int fwk = fw_flag(ct);
    GOT(fwk);
#pragma unroll
    for (int i = 0; i < 4; ++i) {
      const int row = 64 * tg + (lane & 31), c = 2 * wf + (i >> 1), t2 = i & 1;
      xa[i] = (row * 128 + 16 * (h ^ ((row >> 1) & 7))) ^ (32 * i);
      wa[i >> 1][i & 1] = 16384 + 256 * (8 * h + 4 * t2 + q) + 64 * (c ^ q) + 16 * ((2 * blk + (p >> 1)) ^ (2 * h + t2)) + 8 * (p & 1);
      GOT(xa[i]); GOT(wa[i >> 1][i & 1]);
    }
    PIN();
#pragma unroll 1
    for (int bt = 0; bt < nbat; ++bt) {
      const unsigned p0 = pn[0], p1 = pn[1], p2 = pn[2], p3 = pn[3], p4 = pn[4], p5 = pn[5];
      const float lg = bflo(lgn), dsk = bflo(dskn);
      if (bt + 1 < nbat) fetch(bt + 1);
      if (bt) lds_wait_ge(S, CNT + 16 * 5 + 4 * (w ^ 1), bt);
      LAS unsigned* T = (LAS unsigned*)(S + TAB + w * TW) + lane;
      LAS char* Tr = S + TAB + w * TW + g * 16 + (lane & 15) * TROW;
      constexpr int R = TROW / 4;
      const float dt = rbf(__expf(lg)), a = dt * bflo(p0) * 1.4426950408889634f;
      float th = dt * bflo(p1) * 0.15915494309189535f;
      th -= __builtin_rintf(th);
      const f2 e1 = cexpl(a, th, 1.f), e128 = cexpl(a, th, 128.f), e8 = cexpl(a, -th, 8.f);
      const cm m1 = mkm(e1), m128 = mkm(e128), m8 = mkm(e8), m16 = mkm(cmul(e8, m8));
      const f2 e2 = cmul(e1, m1);
      const cm m2 = mkm(e2), m4 = mkm(cmul(e2, m2));
      f2 y = cmul(f2{bflo(p4), -bflo(p5)}, mkm(f2{dt, dt} * f2{bflo(p2), -bflo(p3)})), y2 = cmul(y, m8), t = cexpl(a, th, 1024.f);
#pragma unroll
      for (int i = 0; i < 8; ++i) {
        T[(2 * i) * R] = pk2(y); T[(2 * i + 1) * R] = pk2(y2); T[(16 + i) * R] = pk2(t);
        if (i < 7) { y = cmul(y, m16); y2 = cmul(y2, m16); }
        t = cmul(t, m128);
      }
      T[24 * R] = pk2(t);
      v4u fy[4], fa[4][2];
      f4 ka[3] = {};
#pragma unroll
      for (int ks = 0; ks < 4; ++ks) { fy[ks] = *(LAS v4u*)(Tr + ks * 64); fa[ks][0] = *(LAS v4u*)(Tr + 16 * TROW + ks * 64); }
      PIN();
      f2 u = {1.f, 0.f}, v = e128, z = cmul(e128, m128);
#pragma unroll
      for (int i = 0; i < 8; ++i) {
        T[i * R] = pk2(u); T[(8 + i) * R] = pk2(v);
        if (i < 7) { u = cmul(u, m1); v = cmul(v, m1); }
        if (i < 2) {
          const f2 z2 = cmul(z, m2);
          T[(16 + 4 * i) * R] = pk2(z); T[(17 + 4 * i) * R] = pk2(z2); T[(18 + 4 * i) * R] = pk2(cmul(z, m4)); T[(19 + 4 * i) * R] = pk2(cmul(z2, m4));
        } else if (i < 6) {
          T[(20 + 2 * i) * R] = pk2(z); T[(21 + 2 * i) * R] = pk2(cmul(z, m4));
        }
        if (i < 5) z = cmul(z, m128);
      }
      PIN();
#pragma unroll
      for (int ks = 0; ks < 4; ++ks) ka[2] = __builtin_amdgcn_mfma_f32_16x16x32_bf16(asbf(fa[ks][0]), asbf(fy[ks]), ka[2], 0, 0, 0);
#pragma unroll
      for (int ks = 0; ks < 4; ++ks) { fa[ks][0] = *(LAS v4u*)(Tr + ks * 64); fa[ks][1] = *(LAS v4u*)(Tr + 16 * TROW + ks * 64); }
      PIN();
#pragma unroll
      for (int ks = 0; ks < 4; ++ks)
#pragma unroll
        for (int i = 0; i < 2; ++i) ka[i] = __builtin_amdgcn_mfma_f32_16x16x32_bf16(asbf(fa[ks][i]), asbf(fy[ks]), ka[i], 0, 0, 0);
      if (bt == 0) STAMP(1);
      {
        LAS char* X = S + TAB + w * TW + 4608 + lane * 16;
        *(LAS f4*)X = tg ? ka[0] : ka[1];
        *(LAS f4*)(X + 1024) = ka[2];
        lds_put(CNT + 16 * 4 + 4 * w, bt + 1);
        lds_wait_ge(S, CNT + 16 * 4 + 4 * (w ^ 1), bt + 1);
        LAS char* Y = S + TAB + (w ^ 1) * TW + 4608 + lane * 16;
        f4 km = (tg ? ka[1] : ka[0]) + *(LAS f4*)Y, k2 = ka[2] + *(LAS f4*)(Y + 1024);
        GOT(km); GOT(k2);
        lds_put(CNT + 16 * 5 + 4 * w, bt + 1);
        const int c = lane & 15;
        LAS char* K = S + kar + (2 * bt + wf) * KCH;
        LAS u16* T1 = (LAS u16*)(S + TAB + w * TW);
        const unsigned q01 = pk2(k2.xy), q23 = pk2(k2.zw);
        if (!tg) {
          if (lane == 0) km.x += dsk;
          *(LAS v2u*)(K + 512 + 256 * (g >> 1) + 16 * c + 8 * blk) = v2u{pk2(km.xy), pk2(km.zw)};
          LAS u16* T3 = T1 + 64 * g + c;
          T3[0] = (u16)q01; T3[16] = (u16)(q01 >> 16); T3[32] = (u16)q23; T3[48] = (u16)(q23 >> 16);
          ORDER();
          const unsigned c0_ = T1[lane], c1 = T1[1 + lane], d0 = T1[64 + lane], d1 = T1[65 + lane];
          if (h) *(LAS v4u*)(K + 16 * (lane & 31)) = v4u{0u, 0u, 0u, 0u};
          *(LAS v4u*)(K + 2560 + 16 * lane) = v4u{c0_ * 0x10001u, c0_ * 0x10001u, ((lane & 1) ? c1 : c0_) | (c1 << 16), c1 * 0x10001u};
          *(LAS v4u*)(K + 3584 + 16 * lane) = v4u{d0 * 0x10001u, d0 * 0x10001u, ((lane & 1) ? d1 : d0) | (d1 << 16), d1 * 0x10001u};
        } else {
          const unsigned p01 = pk2(km.xy), p23 = pk2(km.zw);
          if (g < 2) *(LAS v2u*)(T1 + 64 * g + 4 * c) = v2u{p01, p23};
          else { *(LAS unsigned*)(T1 + 64 * g + 2 * c) = p01; *(LAS unsigned*)(T1 + 64 * g + 32 + 2 * c) = p23; }
          if (lane == 0) T1[256] = (u16)q01;
          ORDER();
          const v2u a1 = *(LAS v2u*)(T1 + 4 * (lane & 31));
          const unsigned a4 = T1[4 * (lane & 31) + 4], b0 = *(LAS unsigned*)(T1 + 128 + 2 * lane), b2_ = T1[130 + 2 * lane];
          if (!h) *(LAS v4u*)(K + 1024 + 16 * lane) = v4u{(a1.x & 0xffffu) * 0x10001u, (a1.x >> 16) | (a1.y << 16), (a1.y & 0xffffu) * 0x10001u, (a1.y >> 16) | (a4 << 16)};
          *(LAS v4u*)(K + 1536 + 16 * lane) = v4u{(b0 & 0xffffu) * 0x10001u, b0, (b0 >> 16) * 0x10001u, b2_ * 0x10001u};
        }
      }
    }
    if (cb < 10 && !spare) {
      lds_wait(S, 1);
      v2u xt[8];
#pragma unroll
      for (int m = 0; m < 8; ++m) {
        const int rr = 32 * g + 4 * m + q;
        xt[m] = trd(S + 128 * rr + 16 * ((2 * w + (p >> 1)) ^ ((rr >> 1) & 7)) + 8 * (p & 1));
      }
      LAS char* X = S + TAB + w * TW;
#pragma unroll
      for (int k = 0; k < 4; ++k) *(LAS v4u*)(X + (lane & 15) * 272 + g * 64 + k * 16) = v4u{xt[2 * k].x, xt[2 * k].y, xt[2 * k + 1].x, xt[2 * k + 1].y};
#pragma unroll
      for (int k = 0; k < 4; ++k)
        st16(rws, *(LAS v4u*)(X + (4 * k + g) * 272 + (lane & 15) * 16), WS_XT + (s0 * 64 + 16 * w + 4 * k + g) * 4096 + rp * 256 + (lane & 15) * 16);
    }
    STAMP(2);
    if (!spare) {
      f16v acc[2][2] = {};
      v4u fx[4][2], fw[4][2];
      auto rd = [&](int set, int so, int ks) {
        fw[set][0] = trd2(S + wa[0][0] + so + 4096 * ks, S + wa[0][1] + so + 4096 * ks);
        fx[set][0] = *(LAS v4u*)(S + xa[ks] + so);
        fx[set][1] = *(LAS v4u*)(S + xa[ks] + so + 4096);
        fw[set][1] = trd2(S + wa[1][0] + so + 4096 * ks, S + wa[1][1] + so + 4096 * ks);
      };
      auto mm = [&](int set) {
#pragma unroll
        for (int i = 0; i < 4; ++i) acc[i >> 1][i & 1] = __builtin_amdgcn_mfma_f32_32x32x16_bf16(asbf(fw[set][i >> 1]), asbf(fx[set][i & 1]), acc[i >> 1][i & 1], 0, 0, 0);
#pragma unroll
        for (int i = 0; i < 3; ++i) { __builtin_amdgcn_sched_group_barrier(0x8, 1, 0); __builtin_amdgcn_sched_group_barrier(0x100, 2, 0); }
        __builtin_amdgcn_sched_group_barrier(0x8, 1, 0);
        PIN();
      };
      u64 fw_seen = 0, g0 = 0, g1 = 0, gc = 0;
      unsigned b1v = 0;
      const unsigned o0 = WS_ST + (lane & 7) * 2048 + (rp * 16 + 8 * tg + ((lane & 31) >> 3)) * 8, oc = WS_CS + (c0 + 64 * wf + lane) * 8;
      asm volatile("" : "+v"(acc[0][0]), "+v"(acc[0][1]), "+v"(acc[1][0]), "+v"(acc[1][1]));
      bar();
      STAMP(6);
      tag = rd_tag(S);
      rd(0, 0, 0); rd(1, 0, 1);
      PIN();
#pragma unroll 1
      for (int n = 0; n < 5; ++n) {
        if (n == 4) { fw_seen = ld_hidden(ws, WS_FLAG + 128u * fwk); g0 = ld_hidden(ws, o0); g1 = ld_hidden(ws, o0 + 32); gc = ld_hidden(ws, oc);
            asm volatile("global_load_ushort %0, %1, %2" : "=v"(b1v) : "v"((c0 + 64 * wf + lane) * 2), "s"(b1) : "memory"); }
        rd(2, 0, 2); mm(0);
        rd(3, 0, 3); mm(1);
        rd(0, SLOT, 0); mm(2);
        rd(1, SLOT, 1); mm(3);
        if (n == 4) {
          WAITV(0); GOT(fw_seen); GOT(g0); GOT(g1); GOT(gc); GOT(b1v);
          if (!tag_ok(gc, tag)) gc = wait_granule(ws, oc, tag);
          *(LAS unsigned*)(S + COLT + w * 512 + 256 + lane * 4) = b1v << 16;
          *(LAS unsigned*)(S + COLT + w * 512 + lane * 4) = (unsigned)gc;
          if (!tag_ok(g0, tag)) g0 = wait_granule(ws, o0, tag);
          if (!tag_ok(g1, tag)) g1 = wait_granule(ws, o0 + 32, tag);
          check_flags(ws, fwk, fw_seen, tag);
          PIN();
        } else {
          if (n == 0) WAITV(0);
          bar_raw();
          if (n == 0 && w == 1 && cb < 10 && lane == 0) put_flag(ws, FL_X + rp * 10 + s0, tag);
        }
        rd(2, SLOT, 2); mm(0);
        rd(3, SLOT, 3); mm(1);
#pragma unroll
        for (int i = 0; i < 4; ++i) { xa[i] ^= 2 * SLOT; wa[i >> 1][i & 1] ^= 2 * SLOT; }
        rd(0, 0, 0); mm(2);
        rd(1, 0, 1); mm(3);
        if (n < 4) bar_raw();
      }
      STAMP(3);
      const unsigned hv = WS_HT + (((rp * 8 + 4 * tg + blk) * 54 + (c0 >> 5) + 2 * wf) * 64 + (lane & 15) + 16 * h) * 16 + ((cb == 13 && wf == 0) ? OOB : 0u);
#pragma unroll
      for (int c = 0; c < 2; ++c) {
        f4 cs[4], bb[4];
#pragma unroll
        for (int a = 0; a < 4; ++a) { LAS char* ct_ = S + COLT + w * 512 + (32 * c + 8 * a + 4 * h) * 4; cs[a] = *(LAS f4*)ct_; bb[a] = *(LAS f4*)(ct_ + 256); }
#pragma unroll
        for (int tt = 0; tt < 2; ++tt) {
          const float rs = __builtin_amdgcn_rcpf(bfhi((unsigned)(tt ? g1 : g0))), mr = -bflo((unsigned)(tt ? g1 : g0)) * rs;
          unsigned o[8];
#pragma unroll
          for (int a = 0; a < 4; ++a) {
#pragma unroll
            for (int e = 0; e < 2; ++e) {
              const f2 v = fma2(f2{acc[c][tt][4 * a + 2 * e], acc[c][tt][4 * a + 2 * e + 1]}, f2{rs, rs}, fma2(f2{mr, mr}, e ? cs[a].zw : cs[a].xy, e ? bb[a].zw : bb[a].xy));
              const f2 z = v * fma2(v * v, f2{-0.10294324f, -0.10294324f}, f2{-2.30220819f, -2.30220819f});
              const f2 dn = f2{__builtin_amdgcn_exp2f(z.x), __builtin_amdgcn_exp2f(z.y)} + f2{1.f, 1.f};
              o[2 * a + e] = pk2(v * f2{__builtin_amdgcn_rcpf(dn.x), __builtin_amdgcn_rcpf(dn.y)});
            }
          }
          load_w2_(w2s, NA * (2 * c + tt), NA * (2 * c + tt + 1));
          st16(rws, v4u{o[0], o[1], o[2], o[3]}, hv + (tt * 108 + c) * 1024);
          st16(rws, v4u{o[4], o[5], o[6], o[7]}, hv + (tt * 108 + c) * 1024 + 512);
          PIN();
        }
      }
      STAMP(4);
      lds_put(CNT + 16 * 6 + 4 * wq, 1);
      load_w2_(w2s, 4 * NA, 4 * NA + 8);
      WAITV(8);
      if (lds_arrive(S, 0, wq) && lane == 0) put_flag(ws, FL_H + gi, tag);
      STAMP(5);
      load_w2_(w2s, 4 * NA + 8, 35);
#pragma unroll 1
      for (int j = 0; j < nch; ++j) {
        fh = ld_word(ws, WS_FLAG + 128u * fhk);
        lds_wait_ge(S, CNT + 16 * 2 + 4 * p, j + 1);
        const unsigned pa = (j & 1) * SLOT + (tid >> 5) * 1024 + (((tid >> 1) & 15) ^ (4 * (tid & 1))) * 16 + (tid & 1) * 512;
        f4 lo[4], hi[4], l, m;
#pragma unroll
        for (int v = 0; v < 4; ++v) { lo[v] = *(LAS f4*)(S + pa + v * 8192); hi[v] = *(LAS f4*)(S + (pa ^ 32u) + 256 + v * 8192); }
#pragma unroll
        for (int e = 0; e < 4; ++e) { l[e] = sadd(sadd(lo[0][e], lo[1][e]), sadd(lo[2][e], lo[3][e])); m[e] = sadd(sadd(hi[0][e], hi[1][e]), sadd(hi[2][e], hi[3][e])); }
        GOT(l); GOT(m); GOT(fh);
        lds_put(CNT + 16 * 9 + 4 * wq, 1);
        st16(rws, v4u{pk2(l.xy), pk2(l.zw), pk2(m.xy), pk2(m.zw)}, tid * 16 + WS_YT + (j < 2 ? ch00 + j : ch3) * 4096);
      }
    } else {
      lds_arrive(S, 6, wq);
      lds_wait(S, 6);
      tag = get_tag(S);
#pragma unroll
      for (int j = 0; j < 6; ++j) st16(rws, *(LAS v4u*)(S + KARR + j * KCH + 512 + tid * 16), tid * 16 + WS_K3 + (ch00 + j) * 4096);
      WAITV(0);
      if (lds_arrive(S, 0, wq) && lane == 0) put_flag(ws, FL_K + sp, tag);
      if ((bi >> 3) != 3) warm_w2(4 * (xcd & 1) + (bi >> 3), tag);
      wait_flags(ws, fwk, tag);
      load_w2_(w2s, 0, 35);
    }
    STAMP(7);
  }
  {
    IDS();
    if (w < 4) {
      check_flags(ws, fhk, fh, tag);
      if (w == 0) lds_put(CNT + 16 * 3, 1);
    } else lds_wait_ge(S, CNT + 16 * 3, 1);
    STAMP(8);
    v4u hf[7];
    unsigned hb[4];
#pragma unroll
    for (int pt = 0; pt < 4; ++pt) hb[pt] = WS_HT + ((rt * 4 + ((pt + ct) & 3)) * 54 + w) * 1024;
#pragma unroll
    for (int i = 0; i < 7; ++i) hf[i] = ld16(rws, w2v, hb[0] + (i < 6 ? i * 8192u : k6));
    PIN();
    if (w < 4 && !spare) {
      WAITV(7);
      if (lds_arrive(S, 10, wq) && lane == 0) put_flag(ws, FL_Y + gi, tag);
    }
    bar();
    STAMP(9);
    LAS char* Pw = S + w * GW + g * 256 + (((lane & 15) + g) & 15) * 16;
    const __amdgpu_buffer_rsrc_t rx = __builtin_amdgcn_make_buffer_rsrc((void*)x, 0, L * D * 2, 0x00020000),
                                 ro = __builtin_amdgcn_make_buffer_rsrc(out, 0, L * D * 2, 0x00020000),
                                 rb2 = __builtin_amdgcn_make_buffer_rsrc((void*)b2, 0, D * 2, 0x00020000);
    const int fyk = FL_Y + ct * 28 + (lane < 28 ? lane : 0);
    unsigned tpr[3], tov[3], tyr[3];
    v2u xr[3], bb[3];
    f4 p0[4], s0a;
    v4u yv[2];
    auto task_setup = [&](int i) {
      const int id = i == 0 ? w : i == 1 ? w + 8 : w + 15, pt = id / 5, idx = (id % 5) * 64 + lane, et = idx / 20, eq = idx % 20, t = ((pt + ct) & 3) * 16 + et;
      const bool on = i == 0 || (i == 1 ? w < 7 : w < 5);
      tpr[i] = (pt >= 3 ? 0 : pt) * GBUF + (eq >> 2) * GT + (eq & 3) * 256 + ((et + (eq & 3)) & 15) * 16;
      tov[i] = ((rt * 64 + t) * D + ct * 80 + 4 * eq) * 2 + (on ? 0u : OOB);
      tyr[i] = YT2 + t * YT2ROW + 8 * eq;
      xr[i] = __builtin_amdgcn_raw_buffer_load_b64(rx, tov[i], 0, 0);
      bb[i] = __builtin_amdgcn_raw_buffer_load_b64(rb2, (ct * 80 + 4 * eq) * 2, 0, 0);
    };
    auto bias = [&](int i, f4 sa) { return v2u{pk2(unpk(pk2(sa.xy)) + unpk(bb[i].x)), pk2(unpk(pk2(sa.zw)) + unpk(bb[i].y))}; };
    auto reduce = [&](int i) {
      f4 pr[8];
#pragma unroll
      for (int v = 0; v < 8; ++v) pr[v] = *(LAS f4*)(S + tpr[i] + v * GW);
      const f4 sa = ((pr[0] + pr[1]) + (pr[2] + pr[3])) + ((pr[4] + pr[5]) + (pr[6] + pr[7]));
      return bias(i, sa);
    };
    auto finish = [&](int i, v2u yb) {
      const v2u y = *(LAS v2u*)(S + tyr[i]);
      __builtin_amdgcn_raw_buffer_store_b64(v2u{pk2(unpk(pk2(unpk(xr[i].x) + unpk(y.x))) + unpk(yb.x)), pk2(unpk(pk2(unpk(xr[i].y) + unpk(y.y))) + unpk(yb.y))}, ro, tov[i], 0, 0);
    };
    f4 acc[2][5];
#pragma unroll
    for (int pt = 0; pt < 4; ++pt) {
#pragma unroll
      for (int j = 0; j < 7; ++j) {
        if (pt == 3 && j >= 5) {
          const int c = lane + 64 * (j - 5);
          LAS u16* d = (LAS u16*)(S + YT2 + (c & 7) * 8 * YT2ROW) + 10 * w + (c >> 3);
          if (c < 80) {
#pragma unroll
            for (int e = 0; e < 4; ++e) { d[(2 * e) * (YT2ROW / 2)] = (u16)yv[j - 5][e]; d[(2 * e + 1) * (YT2ROW / 2)] = (u16)(yv[j - 5][e] >> 16); }
          }
        }
        if (pt == 3) {
          auto add4 = [](f4 a, f4 c) { return f4{sadd(a.x, c.x), sadd(a.y, c.y), sadd(a.z, c.z), sadd(a.w, c.w)}; };
          if (j == 2) { s0a = add4(add4(p0[0], p0[1]), add4(p0[2], p0[3])); GOT(s0a); }
          if (j == 4) { s0a = add4(s0a, add4(add4(p0[0], p0[1]), add4(p0[2], p0[3]))); GOT(s0a); lds_put(CNT + 16 * (11 + (w >> 2)) + 4 * wq, 1); }
          if (j == 0 || j == 2) {
#pragma unroll
            for (int v = 0; v < 4; ++v) p0[v] = *(LAS f4*)(S + tpr[0] + (2 * j + v) * GW);
          }
          if (j == 3) { task_setup(1); task_setup(2); }
        }
        if (j >= 1 && j < 6 && pt >= 1) *(LAS f4*)(Pw + (pt - 1) * GBUF + (j - 1) * GT) = acc[(pt - 1) & 1][j - 1];
#pragma unroll
        for (int n = 0; n < 5; ++n)
          acc[pt & 1][n] = __builtin_amdgcn_mfma_f32_16x16x32_bf16(asbf(wf2[5 * j + n]), asbf(hf[j]), j ? acc[pt & 1][n] : f4{0.f, 0.f, 0.f, 0.f}, 0, 0, 0);
        if (pt < 3) hf[j] = ld16(rws, w2v, hb[pt + 1] + (j < 6 ? j * 8192u : k6));
        PIN();
        if (pt == 1 && j == 3) fh = ld_hidden(ws, WS_FLAG + 128u * fyk);
        if (pt == 2 && j == 3) {
          WAITV(7);
          GOT(fh);
          check_flags(ws, fyk, fh, tag);
#pragma unroll
          for (int k = 0; k < 2; ++k) {
            const int c = lane + 64 * k;
            yv[k] = ld16(rws, (10 * w + (c >> 3)) * 4096 + (c & 7) * 16 + (c < 80 ? 0u : OOB), WS_YT + ct * 80 * 4096 + rt * 128);
          }
          PIN();
        }
      }
      if (pt == 0) STAMP(11);
      if (pt == 1) STAMP(14);
      if (pt == 1) { task_setup(0); PIN(); }
      if (pt == 2) bar();
    }
    if (w < 4) STAMP(12);
    lds_wait_ge(S, CNT + 16 * 11 + 4 * (lane & 7), 1);
#pragma unroll
    for (int n = 0; n < 5; ++n) *(LAS f4*)(Pw + n * GT) = acc[1][n];
    bar();
    if (w < 4) STAMP(13);
    finish(0, bias(0, s0a));
    const v2u y1 = reduce(1), y2 = reduce(2);
    finish(1, y1);
    finish(2, y2);
  }
  STAMP(10);
  STAMP_OUT();
  if (b == 0 && w == 7) {
#pragma unroll 1
    for (unsigned s = 0, j = 0; s < SPIN_CAP && j < 4; ++s)
      if (__all((unsigned)(ld_word(ws, WS_ST + (64 * j + lane) * 8) >> 32) == (unsigned)tag)) ++j;
    if (lane == 0) __hip_atomic_store((GAS u64*)ws, tag, __ATOMIC_RELAXED, __HIP_MEMORY_SCOPE_AGENT);
  }
  asm volatile("s4_code_end:");
}
}

extern "C" void kernel_launch(void* const* d_in, const int* in_sizes, int n_in, void* d_out, int out_size, void* d_ws, size_t ws_size,
                              hipStream_t stream) {
  (void)in_sizes; (void)n_in; (void)out_size; (void)ws_size;
  (void)hipFuncSetAttribute((const void*)s4mlp_kernel, hipFuncAttributeMaxDynamicSharedMemorySize, LDS_BYTES);
  s4mlp_kernel<<<256, 512, LDS_BYTES, stream>>>((const u16*)d_in[0], (const u16*)d_in[1], (const u16*)d_in[2], (const u16*)d_in[3], (const u16*)d_in[4],
                                                (const u16*)d_in[5], (const u16*)d_in[6], (const u16*)d_in[11], (const u16*)d_in[7], (const u16*)d_in[8],
                                                (const u16*)d_in[10], (const u16*)d_in[12], (const u16*)d_in[13], (u16*)d_out, (char*)d_ws);
}
```

```cpp
#include <hip/hip_runtime.h>
#include <hip/hip_bf16.h>
#include <stdint.h>

namespace {
typedef unsigned short u16;
typedef unsigned long long u64;
typedef __bf16 bf8 __attribute__((ext_vector_type(8)));
typedef __bf16 bf2 __attribute__((ext_vector_type(2)));
typedef short s4 __attribute__((ext_vector_type(4)));
typedef float f16v __attribute__((ext_vector_type(16)));
typedef float f4 __attribute__((ext_vector_type(4)));
typedef float f2 __attribute__((ext_vector_type(2)));
typedef unsigned v4u __attribute__((ext_vector_type(4)));
typedef unsigned v2u __attribute__((ext_vector_type(2)));
#define LAS __attribute__((address_space(3)))
#define GAS __attribute__((address_space(1)))
#define DI __device__ __forceinline__

constexpr int L = 2048, D = 640, N = 128, F = 1728;
constexpr unsigned WS_END0 = (1u << 20) + 168u * 54 * 1024 + 2 * 640u * 4096;
constexpr unsigned WS_FLAG = 4096;
constexpr int FL_H = 0, FL_X = 224, FL_Y = 384, FL_K = 608, FL_W = 640;
constexpr unsigned WS_ST = 256u << 10;
constexpr unsigned WS_CS = WS_ST + 2048 * 8;
constexpr unsigned WS_K3 = WS_END0;
constexpr unsigned WS_HT = 1u << 20;
constexpr unsigned WS_W2T = WS_HT + 128u * 54 * 1024;
constexpr unsigned WS_XT = WS_W2T + 40u * 54 * 1024;
constexpr unsigned WS_YT = WS_XT + 640u * 4096;
constexpr unsigned WS_END = WS_YT + 2 * 640u * 4096;
static_assert(WS_K3 == WS_YT + 640u * 4096, "workspace map");
constexpr unsigned WS_DBG = 32u << 20;
constexpr unsigned OOB = 0x40000000u;
constexpr int LDS_BYTES = 163840;
constexpr int SLOT = 32768;
constexpr int TAB = 98304, TROW = 272, TW = 32 * TROW;
constexpr int CNT = TAB + 4 * TW, TAGL = CNT + 240;
constexpr int COLT = CNT + 256;
constexpr int W2U_G = COLT + 2048, W2U_S = 16384;
constexpr int KCH = 4608, KARR = LDS_BYTES - 6 * KCH;
constexpr int UCP = 65536, UST = 4160, UCH = 4 * UST;
constexpr int GT = 1024, GW = 5 * GT, GBUF = 8 * GW, YT2 = CNT + 256, YT2ROW = 168;
static_assert(W2U_G + 4096 <= KARR + 3 * KCH && UCP + 3 * UCH <= CNT && 3 * GBUF <= CNT && YT2 + 64 * YT2ROW <= LDS_BYTES, "LDS map");
#define NB1 20
#define NB0 8
#define WARM 0
#define NA 0
#define CPRIO 1
#define FYJ 6
#define FXN 1
#define BPRIO 1
#define SKEW 1
#define MAP48 1
#define LAUX 16
constexpr unsigned SPIN_CAP = 1u << 17;
#define SLP 1

DI float bflo(unsigned v) { return __uint_as_float(v << 16); }
DI float bfhi(unsigned v) { return __uint_as_float(v & 0xffff0000u); }
DI f2 unpk(unsigned v) { return f2{bflo(v), bfhi(v)}; }
DI unsigned pk2(f2 p) { return __builtin_bit_cast(unsigned, __builtin_convertvector(p, bf2)); }
DI unsigned pk(float a, float b) { return pk2(f2{a, b}); }
DI float rbf(float a) { return bflo(pk(a, a)); }
DI bf8 asbf(v4u v) { return __builtin_bit_cast(bf8, v); }
DI v4u mk_srd(const void* p, unsigned bytes) { u64 a = (u64)p; return v4u{(unsigned)a, (unsigned)(a >> 32) & 0xffffu, bytes, 0x00020000u}; }
#define SET_M0(v) asm volatile("s_mov_b32 m0, %0\n\ts_nop 0" ::"s"(__builtin_amdgcn_readfirstlane(v)) : "memory")
#define DMA(srd, voff, soff, imm) asm volatile("buffer_load_dwordx4 %0, %1, %2 offen offset:" #imm " lds" ::"v"(voff), "s"(srd), "s"(soff) : "memory")
#define WAITV(n) asm volatile("s_waitcnt vmcnt(" #n ")" ::: "memory")
#define PIN() __builtin_amdgcn_sched_barrier(0)
#define ORDER() asm volatile("" ::: "memory")
DI void bar() { asm volatile("s_waitcnt lgkmcnt(0)\n\ts_barrier" ::: "memory"); }
DI void bar_raw() { PIN(); asm volatile("s_barrier" ::: "memory"); PIN(); }
DI v2u trd(LAS char* p) { return __builtin_bit_cast(v2u, __builtin_amdgcn_ds_read_tr16_b64_v4i16((LAS s4*)p)); }
DI v4u trd2(LAS char* p0, LAS char* p1) { v2u a = trd(p0), b = trd(p1); return v4u{a.x, a.y, b.x, b.y}; }
DI float dot2(unsigned a, unsigned b, float c) { return __builtin_amdgcn_fdot2_f32_bf16(__builtin_bit_cast(bf2, a), __builtin_bit_cast(bf2, b), c, false); }
DI float dot8(v4u a, v4u b, float c) { return dot2(a.w, b.w, dot2(a.z, b.z, dot2(a.y, b.y, dot2(a.x, b.x, c)))); }
DI float sum8(v4u a, float c) { const unsigned o = 0x3f803f80u; return dot2(a.w, o, dot2(a.z, o, dot2(a.y, o, dot2(a.x, o, c)))); }
DI f2 fma2(f2 a, f2 b, f2 c) { return __builtin_elementwise_fma(a, b, c); }
struct cm { f2 p, n; };
DI cm mkm(f2 b) { return cm{b, f2{-b.y, b.x}}; }
DI f2 cmul(f2 a, cm b) { return fma2(a.yy, b.n, a.xx * b.p); }
DI f2 cexpl(float a, float th, float l) {
  const float m = __builtin_amdgcn_exp2f(a * l), rv = __builtin_amdgcn_fractf(th * l);
  return f2{m, m} * f2{__builtin_amdgcn_cosf(rv), __builtin_amdgcn_sinf(rv)};
}
DI float xor32(float v) { const auto r = __builtin_amdgcn_permlane32_swap(__float_as_uint(v), __float_as_uint(v), false, false); return __uint_as_float(r[0]) + __uint_as_float(r[1]); }
DI void st16(__amdgpu_buffer_rsrc_t r, v4u v, unsigned off) { __builtin_amdgcn_raw_buffer_store_b128(v, r, off, 0, 16); }
DI v4u ld16(__amdgpu_buffer_rsrc_t r, unsigned voff, unsigned soff) { return __builtin_amdgcn_raw_buffer_load_b128(r, voff, soff, LAUX); }
DI void put_flag(char* ws, int k, u64 tag) { __hip_atomic_store((GAS u64*)(ws + WS_FLAG + 128u * k), tag, __ATOMIC_RELAXED, __HIP_MEMORY_SCOPE_AGENT); }
DI u64 ld_word(char* ws, unsigned off) { return __hip_atomic_load((GAS u64*)(ws + off), __ATOMIC_RELAXED, __HIP_MEMORY_SCOPE_AGENT); }
DI void wait_flags(char* ws, int k, u64 tag) {
#pragma unroll 1
  for (unsigned s = 0; s < SPIN_CAP; ++s) {
    if (__all(ld_word(ws, WS_FLAG + 128u * k) == tag)) break;
    __builtin_amdgcn_s_sleep(1);
  }
  __builtin_amdgcn_fence(__ATOMIC_ACQUIRE, "wavefront");
}
DI u64 next_tag(u64 ep) { return ep * 6364136223846793005ull + 1442695040888963407ull; }
DI float ld_bf_uniform(const u16* p, int i) { const unsigned v = ((const unsigned*)p)[i >> 1]; return (i & 1) ? bfhi(v) : bflo(v); }
DI void lds_put(unsigned addr, unsigned v) { asm volatile("s_mov_b64 exec, 1\n\tds_write_b32 %0, %1\n\ts_mov_b64 exec, -1" ::"v"(addr), "v"(v) : "memory"); }
DI bool lds_arrive(LAS char* S, int c, int q) {
  asm volatile("s_waitcnt lgkmcnt(0)" ::: "memory");
  lds_put(CNT + 16 * c + 4 * q, 1u);
  const v4u v = *(volatile LAS v4u*)(S + CNT + 16 * c);
  return __builtin_amdgcn_readfirstlane(v.x & v.y & v.z & v.w);
}
DI void lds_wait(LAS char* S, int c) {
#pragma unroll 1
  for (unsigned s = 0; s < (SPIN_CAP << 4); ++s) {
    const v4u v = *(volatile LAS v4u*)(S + CNT + 16 * c);
    if (__builtin_amdgcn_readfirstlane(v.x & v.y & v.z & v.w)) break;
    __builtin_amdgcn_s_sleep(SLP);
  }
  ORDER();
}
DI void lds_set(LAS char* S, int off, unsigned v) { asm volatile("s_waitcnt lgkmcnt(0)" ::: "memory"); lds_put(off, v); }
DI unsigned lds_get(LAS char* S, unsigned off) { return *(volatile LAS unsigned*)(S + off); }
DI void lds_wait_ge(LAS char* S, unsigned off, unsigned n) {
#pragma unroll 1
  for (unsigned s = 0; s < (SPIN_CAP << 4); ++s) {
    if (__all(lds_get(S, off) >= n)) break;
    __builtin_amdgcn_s_sleep(SLP);
  }
  ORDER();
}
DI u64 rd_tag(LAS char* S) { const v2u t = *(volatile LAS v2u*)(S + TAGL); return ((u64)(unsigned)__builtin_amdgcn_readfirstlane(t.y) << 32) | (unsigned)__builtin_amdgcn_readfirstlane(t.x); }
DI u64 get_tag(LAS char* S) { lds_wait_ge(S, TAGL + 8, 1); return rd_tag(S); }
DI u64 ld_hidden(char* ws, unsigned off) { u64 v; asm volatile("global_load_dwordx2 %0, %1, %2 sc1" : "=v"(v) : "v"(off), "s"(ws) : "memory"); return v; }
DI float sadd(float a, float b) { float r; asm("v_add_f32 %0, %1, %2" : "=v"(r) : "v"(a), "v"(b)); return r; }
DI float smul(float a, float b) { float r; asm("v_mul_f32 %0, %1, %2" : "=v"(r) : "v"(a), "v"(b)); return r; }
DI float sfms(float a, float b, float c) { float r; asm("v_fma_f32 %0, %1, %2, -%3" : "=v"(r) : "v"(a), "v"(b), "v"(c)); return r; }
DI float ssub(float a, float b) { float r; asm("v_sub_f32 %0, %1, %2" : "=v"(r) : "v"(a), "v"(b)); return r; }
DI bool tag_ok(u64 g, u64 tag) { return __all((unsigned)(g >> 32) == (unsigned)tag); }
DI u64 wait_granule(char* ws, unsigned off, u64 tag) {
  u64 g = 0;
#pragma unroll 1
  for (unsigned s = 0; s < SPIN_CAP; ++s) { g = ld_word(ws, off); if (tag_ok(g, tag)) break; }
  return g;
}
#define GOT(v) asm volatile("" : "+v"(v))
DI void check_flags(char* ws, int k, u64 seen, u64 tag) {
  if (!__all(seen == tag)) wait_flags(ws, k, tag);
  __builtin_amdgcn_fence(__ATOMIC_ACQUIRE, "wavefront");
}
#define STAMP(k)
#define STAMP_DECL
#define STAMP_OUT()

__global__ __launch_bounds__(512, 2) void s4mlp_kernel(const u16* __restrict__ x, const u16* __restrict__ Are, const u16* __restrict__ Aim,
                                                      const u16* __restrict__ Bre, const u16* __restrict__ Bim, const u16* __restrict__ Cre,
                                                      const u16* __restrict__ Cim, const u16* __restrict__ b1, const u16* __restrict__ logdt,
                                                      const u16* __restrict__ dskip, const u16* __restrict__ w1, const u16* __restrict__ w2,
                                                      const u16* __restrict__ b2, u16* __restrict__ out, char* ws) {
  LAS char* const S = (LAS char*)0;
  const int tid = threadIdx.x, lane = tid & 63, w = __builtin_amdgcn_readfirstlane(tid >> 6), wq = w & 3;
  const int b = blockIdx.x;
  STAMP_DECL;
  STAMP(0);
  v4u pf0 = {}, pf1 = {};
  if (w == 7) {
    unsigned len;
    asm volatile("s4_code_begin:\n\ts_mov_b32 %0, s4_code_end-s4_code_begin-256" : "=s"(len));
    const v4u sc = mk_srd((const void*)__builtin_amdgcn_s_getpc(), __builtin_amdgcn_readfirstlane(len));
    asm volatile("s_nop 4\n\tbuffer_load_dwordx4 %0, %2, %3, 0 offen\n\tbuffer_load_dwordx4 %1, %4, %3, 0 offen"
                 : "=&v"(pf0), "=&v"(pf1) : "v"((b >> 3) * 1024 + lane * 16), "s"(sc), "v"((b >> 3) * 1024 + lane * 16 + 32768) : "memory");
    *(LAS unsigned*)(S + CNT + 4 * lane) = 0u;
  }
  asm volatile("" ::"s"(x), "s"(w1), "s"(ws), "s"(Are), "s"(Aim), "s"(Bre), "s"(Bim), "s"(Cre), "s"(Cim), "s"(w2), "s"(logdt), "s"(dskip));
  bar();
#define IDS()                                                                                                              \
  int bl = b;                                                                                                              \
  asm volatile("" : "+s"(bl));                                                                                             \
  bl = __builtin_amdgcn_readfirstlane(bl);                                                                                 \
  const int xcd = bl & 7, bi = bl >> 3;                                                                                    \
  const int rp = 4 * (xcd >> 1) + (bi >> 3), cb = 7 * (xcd & 1) + (bi & 7);                                 \
  const bool spare = (bi & 7) == 7;                                                                       \
  const int c0 = cb * 128 > F - 128 ? F - 128 : cb * 128, gi = rp * 14 + cb, sp = xcd * 4 + (bi >> 3);                      \
  const int gq = gi / 28, gm = gi % 28;   \
  const int ch00 = spare ? 80 * (sp >> 2) + 56 + 6 * (sp & 3) : 80 * gq + 2 * gm, ch3 = 80 * gq + 56 + gm;                   \
  const int nbat = spare ? 3 : 1, nch = gm < 24 ? 3 : 2;                                                                   \
  const int rt = MAP48 ? 4 * xcd + (bi >> 3) : 8 * (xcd >> 1) + (bi >> 2), ct = MAP48 ? (bi & 7) : 4 * (xcd & 1) + (bi & 3);                                 \
  const int kar = spare ? KARR : KARR + 3 * KCH;                                                                             \
  const int s0 = SKEW == 0 ? 0 : SKEW == 1 ? (cb * 3 + rp * 5 + (rp >> 1)) % 10 : SKEW == 2 ? 5 * ((cb + rp) & 1) : (xcd * 5 + (xcd >> 1)) % 10;             \
  const unsigned w2s = WS_W2T + (ct * 270 + w) * 1024;                                                                      \
  const int fhk = FL_H + (rt >> 1) * 14 + (lane < 14 ? lane : 0);                                                          \
  const unsigned hb0 = WS_HT + ((rt * 4 + (ct & 3)) * 54 + w) * 1024;                                                      \
  (void)c0; (void)ch3; (void)nch; (void)ch00; (void)nbat; (void)kar; (void)s0; (void)w2s; (void)fhk; (void)hb0
  const __amdgpu_buffer_rsrc_t rws = __builtin_amdgcn_make_buffer_rsrc(ws, 0, WS_END, 0x00020000);
  const int h = lane >> 5, g = lane >> 4, q = (lane & 15) >> 2, p = lane & 3, blk = g & 1;
  auto fw_flag = [&](int ct) { const int k = (lane >> 1) * 8 + w; return FL_W + (k < 54 ? k : w) * 10 + ((ct * 80 + (lane & 1) * 79) >> 6); };
  const unsigned k6 = w < 6 ? 49152u : 0u;
  v4u wf2[35];
  const unsigned w2v = lane * 16, w2v6 = w2v + (w < 6 ? 0u : OOB);
  auto load_w2_ = [&](unsigned w2s, int i0, int i1) {
#pragma unroll
    for (int i = 0; i < 35; ++i)
      if (i >= i0 && i < i1) wf2[i] = ld16(rws, i >= 30 ? w2v6 : w2v, w2s + (i % 5) * (54 * 1024) + (i / 5) * 8192);
  };
  auto warm_w2 = [&](int c2, u64 tag) {
    wait_flags(ws, fw_flag(c2), tag);
    load_w2_(WS_W2T + (c2 * 270 + w) * 1024, 0, 35);
#pragma unroll
    for (int i = 0; i < 35; ++i) GOT(wf2[i]);
  };
  v4u hf[7];
  auto load_h = [&](unsigned base) {
#pragma unroll
    for (int i = 0; i < 7; ++i) hf[i] = ld16(rws, w2v, base + (i < 6 ? i * 8192u : k6));
  };
  u64 tag, fh = 0;
  if (w >= 4) {
    IDS();
    int sn = s0;
    const int u0 = spare ? 448 + 3 * sp : 2 * gi;
    const v4u sx = mk_srd(x, L * D * 2), sw1 = mk_srd(w1, D * F * 2);
    unsigned vx[4], vw[4];
    auto ring_setup = [&]() {
      int ln = lane;
      asm volatile("" : "+v"(ln));
#pragma unroll
      for (int j = 0; j < 4; ++j) {
        const int rx = 8 * j + (ln >> 3);
        vx[j] = (rp * 128 + rx) * (D * 2) + 16 * ((ln & 7) ^ ((rx >> 1) & 7)) - 1024 * j;
        vw[j] = (4 * j + (ln >> 4)) * (F * 2) + c0 * 2 + 16 * ((ln & 15) ^ (((ln >> 4) << 2) | j)) - 1024 * j;
      }
    };
    auto quarter = [&](int st, int slot, int k) {
      const unsigned so = st * 128 + k * (32 * D * 2), sv = st * (64 * F * 2) + k * (16 * F * 2);
      SET_M0(slot * SLOT + k * 4096);
      DMA(sx, vx[0], so, 0); DMA(sx, vx[1], so, 1024); DMA(sx, vx[2], so, 2048); DMA(sx, vx[3], so, 3072);
      SET_M0(slot * SLOT + 16384 + k * 4096);
      DMA(sw1, vw[0], sv, 0); DMA(sw1, vw[1], sv, 1024); DMA(sw1, vw[2], sv, 2048); DMA(sw1, vw[3], sv, 3072);
    };
    if (w == 7) {
      const __amdgpu_buffer_rsrc_t rx = __builtin_amdgcn_make_buffer_rsrc((void*)x, 0, L * D * 2, 0x00020000),
                                   rw2 = __builtin_amdgcn_make_buffer_rsrc((void*)w2, 0, D * F * 2, 0x00020000);
      const u64 ep = ld_word(ws, 0);
      v4u xv[10], wv[3][4];
#pragma unroll
      for (int i = 0; i < 10; ++i) xv[i] = __builtin_amdgcn_raw_buffer_load_b128(rx, (lane >> 3) * (D * 2) + (lane & 7) * 16 + i * 128, b * (8 * D * 2), 0);
#pragma unroll
      for (int k = 0; k < 3; ++k) {
        const int u = u0 + k;
        const unsigned vo = (lane >> 3) * (D * 2) + (lane & 7) * 16 + ((k < 2 || spare) && u < 540 ? 0u : OOB);
#pragma unroll
        for (int i = 0; i < 4; ++i) wv[k][i] = __builtin_amdgcn_raw_buffer_load_b128(rw2, vo, ((u / 10) * 32 + 8 * i) * (D * 2) + (u % 10) * 128, 0);
      }
      PIN();
      ring_setup();
      const u64 tg = next_tag(ep);
      lds_put(TAGL, (unsigned)tg); lds_put(TAGL + 4, (unsigned)(tg >> 32));
      lds_set(S, TAGL + 8, 1);
      GOT(pf0); GOT(pf1);
      float sm[2] = {}, sq[2] = {};
#pragma unroll
      for (int i = 0; i < 10; ++i) { sm[i & 1] = sum8(xv[i], sm[i & 1]); sq[i & 1] = dot8(xv[i], xv[i], sq[i & 1]); }
      float sum = sm[0] + sm[1], ssq = sq[0] + sq[1];
#pragma unroll
      for (int m = 1; m < 8; m <<= 1) { sum += __shfl_xor(sum, m); ssq += __shfl_xor(ssq, m); }
      const float mean = sum * (1.f / D), var = fmaxf(ssq * (1.f / D) - mean * mean, 0.f);
      const unsigned ms = pk(mean, __builtin_amdgcn_sqrtf(rbf(rbf(var) + 1e-5f)));
      if ((lane & 7) == 0) __hip_atomic_store((GAS u64*)(ws + WS_ST + (lane >> 3) * 2048 + b * 8), (tg << 32) | ms, __ATOMIC_RELAXED, __HIP_MEMORY_SCOPE_AGENT);
      STAMP(1);
      LAS char* U = S + (spare ? W2U_S : W2U_G);
      LAS char* B = U + (4 * (blk + 4 * (g >> 1)) + q) * 128 + 8 * p;
      GOT(wv[2][0]); GOT(wv[2][1]); GOT(wv[2][2]); GOT(wv[2][3]);
#pragma unroll
      for (int k = 0; k < 3; ++k) {
        const int u = u0 + k;
        if ((k < 2 || spare) && u < 540) {
#pragma unroll
          for (int i = 0; i < 4; ++i) *(LAS v4u*)(U + i * 1024 + lane * 16) = wv[k][i];
#pragma unroll
          for (int n = 0; n < 4; ++n) st16(rws, trd2(B + 32 * n, B + 32 * n + 1024), lane * 16 + WS_W2T + (((u % 10) * 4 + n) * 54 + u / 10) * 1024);
        }
      }
    } else if (!spare) {
      ring_setup();
#pragma unroll 1
      for (int t = wq; t < 12; t += 3) {
        if (t >= 9) { WAITV(8); lds_put(CNT + 28, 1); lds_put(CNT + 16 + 4 * wq, 1); }
        quarter(sn + (t >> 2) >= 10 ? sn + (t >> 2) - 10 : sn + (t >> 2), t >> 2, t & 3);
      }
      WAITV(8);
      STAMP(1);
    } else if (sp < 27) {
      const __amdgpu_buffer_rsrc_t rw1 = __builtin_amdgcn_make_buffer_rsrc((void*)w1, 0, D * F * 2, 0x00020000);
      v4u v[27];
#pragma unroll
      for (int i = 0; i < 27; ++i) v[i] = __builtin_amdgcn_raw_buffer_load_b128(rw1, (216 * wq + 8 * i + (lane >> 3)) * (F * 2) + (lane & 7) * 16, sp * 128, 0);
      PIN();
      f2 a[4] = {};
#pragma unroll
      for (int i = 0; i < 27; ++i)
#pragma unroll
        for (int e = 0; e < 4; ++e) a[e] += unpk(v[i][e]);
      *(LAS f4*)(S + (wq * 64 + lane) * 32) = f4{a[0].x, a[0].y, a[1].x, a[1].y};
      *(LAS f4*)(S + (wq * 64 + lane) * 32 + 16) = f4{a[2].x, a[2].y, a[3].x, a[3].y};
      lds_put(CNT + 16 + 12, 1);
      if (lds_arrive(S, 1, wq)) {
        float t = 0.f;
#pragma unroll
        for (int i = 0; i < 24; ++i) t += *(LAS float*)(S + i * 256 + lane * 4);
        __hip_atomic_store((GAS u64*)(ws + WS_CS + (sp * 64 + lane) * 8), (get_tag(S) << 32) | __float_as_uint(t), __ATOMIC_RELAXED, __HIP_MEMORY_SCOPE_AGENT);
      }
    }
    int fwk = fw_flag(ct);
    GOT(fwk);
    STAMP(2);
    if (spare) {
      tag = get_tag(S);
      if (w == 7) {
        WAITV(0);
        if (lane < 3 && u0 + lane < 540) put_flag(ws, FL_W + u0 + lane, tag);
      }
      if (MAP48) { warm_w2(2 * (bi >> 3), tag); if ((bi >> 3) != 3) warm_w2(2 * (bi >> 3) + 1, tag); } else if ((bi >> 3) != 3) warm_w2(4 * (xcd & 1) + (bi >> 3), tag);
      wait_flags(ws, fwk, tag);
      load_w2_(w2s, 0, 35);
    } else {
      const int t4 = tid - 256;
      const int fxk = FL_X + (lane & 15) * 10 + ((g == 2 && nch == 3 ? ch3 : ch00 + (g == 1)) >> 6);
      bar();
      STAMP(6);
      tag = rd_tag(S);
      sn = sn >= 7 ? sn - 7 : sn + 3;
      auto next = [&](int slot) { quarter(sn, slot, wq); sn = sn == 9 ? 0 : sn + 1; };
      next(3);
      WAITV(8);
      if (w == 7 && lane < 2) put_flag(ws, FL_W + u0 + lane, tag);
      bar_raw();
#pragma unroll 1
      for (int n = 0; n < 4; ++n) {
        next(n);
        WAITV(8);
        bar_raw();
      }
      next(0);
      u64 px = ld_hidden(ws, WS_FLAG + 128u * fxk);
      WAITV(9);
      bar_raw();
      STAMP(3);
      next(1);
      WAITV(8);
      bar_raw();
      STAMP(4);
      GOT(px);
      check_flags(ws, fxk, px, tag);
      v2u gr[8];
      v4u un[3];
#pragma unroll
      for (int i = 0; i < 8; ++i) gr[i] = __builtin_amdgcn_raw_buffer_load_b64(rws, t4 * 8, WS_ST + i * 2048, 16);
#pragma unroll
      for (int j = 0; j < 3; ++j) un[j] = ld16(rws, t4 * 16 + (j < nch ? 0u : OOB), WS_XT + (j < 2 ? ch00 + j : ch3) * 4096);
      u64 pw = ld_hidden(ws, WS_FLAG + 128u * fwk);
      WAITV(12);
      bar_raw();
      STAMP(5);
      __builtin_amdgcn_s_setprio(BPRIO);
      {
        unsigned bad = 0;
#pragma unroll
        for (int i = 0; i < 8; ++i) bad |= gr[i].y ^ (unsigned)tag;
        if (!__all(bad == 0)) {
#pragma unroll
          for (int i = 0; i < 8; ++i) gr[i].x = (unsigned)wait_granule(ws, WS_ST + i * 2048 + t4 * 8, tag);
        }
        float rr[8];
#pragma unroll
        for (int e = 0; e < 8; ++e) rr[e] = __builtin_amdgcn_rcpf(bfhi(gr[e].x));
        asm volatile("s_waitcnt vmcnt(0)" : "+v"(rr[0]), "+v"(rr[1]), "+v"(rr[2]), "+v"(rr[3]), "+v"(rr[4]), "+v"(rr[5]), "+v"(rr[6]), "+v"(rr[7]), "+v"(pw), "+v"(un[0]), "+v"(un[1]), "+v"(un[2])::"memory");
        check_flags(ws, fwk, pw, tag);
        load_w2_(w2s, 0, NB0);
        PIN();
        STAMP(1);
#pragma unroll
        for (int e = 0; e < 4; ++e) {
          const float n0 = smul(bflo(gr[2 * e].x), rr[2 * e]), n1 = smul(bflo(gr[2 * e + 1].x), rr[2 * e + 1]);
#pragma unroll
          for (int j = 0; j < 3; ++j) un[j][e] = pk(sfms(bflo(un[j][e]), rr[2 * e], n0), sfms(bfhi(un[j][e]), rr[2 * e + 1], n1));
        }
        LAS char* U = S + UCP + 32 + t4 * 16;
        if (t4 < 56) *(LAS v4u*)(S + (t4 < 24 ? UCP + (t4 >> 1) * UST + (t4 & 1) * 16 : KARR + 5 * KCH + (t4 - 24) * 16)) = v4u{0u, 0u, 0u, 0u};
#pragma unroll
        for (int j = 0; j < 3; ++j) *(LAS v4u*)(U + j * UCH) = un[j];
        lds_put(CNT + 16 * 7 + 4 * wq, 1);
        lds_wait(S, 7);
#pragma unroll
        for (int j = 0; j < 3; ++j) {
          const v4u d = un[j]; const v2u pv = *(LAS v2u*)(U + j * UCH - 8);
          const unsigned a0 = __builtin_amdgcn_alignbit(pv.y, pv.x, 16), a1 = __builtin_amdgcn_alignbit(d.x, pv.y, 16), a2 = __builtin_amdgcn_alignbit(d.y, d.x, 16),
                         a3 = __builtin_amdgcn_alignbit(d.z, d.y, 16), a4 = __builtin_amdgcn_alignbit(d.w, d.z, 16);
          *(LAS v4u*)(U + j * UCH + UST) = v4u{a1, a2, a3, a4};
          *(LAS v4u*)(U + j * UCH + 2 * UST) = v4u{pv.y, d.x, d.y, d.z};
          *(LAS v4u*)(U + j * UCH + 3 * UST) = v4u{a0, a1, a2, a3};
        }
      }
      if (nch == 3) wait_flags(ws, FL_K + 4 * gq + gm / 6, tag);
      const v4u k3 = ld16(rws, t4 * 16, WS_K3 + ch3 * 4096);
      ORDER();
      load_w2_(w2s, NB0, NB1);
      *(LAS v4u*)(S + KARR + 5 * KCH + 512 + t4 * 16) = k3;
      lds_put(CNT + 16 * 8 + 4 * wq, 1);
      STAMP(12);
      LAS char* Kp = S + kar + 512 + 32 * ((lane & 15) - (g >> 1)) + 16 * blk - 64 * wq - 256;
      LAS char* Up = S + UCP + q * UST + 32 + 32 * (g >> 1) - 16 * blk + 8 * p + 64 * wq - 8;
      LAS char* const Pp0 = S + wq * 8192 + g * 256 + (((lane & 15) ^ (2 * g)) * 16);
      lds_wait(S, 8);
      v4u kf[8], kg[8], ua[2], ub[2];
      auto rd_u = [&](int o, int s) {
        ua[s & 1] = trd2(Up + o * UCH + 512 * s + 8, Up + o * UCH + 512 * s); ub[s & 1] = trd2(Up + o * UCH + 512 * (7 - s) + 264, Up + o * UCH + 512 * (7 - s) + 256);
        kg[s] = *(LAS v4u*)(Kp + o * KCH + 512 * s);
      };
      rd_u(0, 0);
#pragma unroll
      for (int e = 7; e >= 0; --e) kf[e] = *(LAS v4u*)(Kp + 512 * e + 256);
      __builtin_amdgcn_s_setprio(1);
      lds_wait(S, 6);
      STAMP(13);
      auto channel = [&](int cj, bool last) {
        f4 acc[8];
        PIN();
#pragma unroll
        for (int s = 0; s < 8; ++s) {
          if (s < 7) rd_u(0, s + 1);
          if (last) load_w2_(w2s, NB1 + 2 * s, NB1 + 2 * s + 2);
          else if (s > 0) kf[8 - s] = *(LAS v4u*)(Kp + KCH + 512 * (8 - s) + 256);
#pragma unroll
          for (int R = 7; R >= s; --R) acc[R] = __builtin_amdgcn_mfma_f32_16x16x32_bf16(asbf(ua[s & 1]), asbf(kf[R - s]), s ? acc[R] : f4{0.f, 0.f, 0.f, 0.f}, 0, 0, 0);
#pragma unroll
          for (int R = 7 - s; R < 8; ++R) acc[R] = __builtin_amdgcn_mfma_f32_16x16x32_bf16(asbf(ub[s & 1]), asbf(kg[R - 7 + s]), acc[R], 0, 0, 0);
#pragma unroll
          for (int i = 0; i < 7; ++i) { __builtin_amdgcn_sched_group_barrier(0x8, 1, 0); __builtin_amdgcn_sched_group_barrier(0x90, 1, 0); }
          PIN();
        }
        if (!last) {
          kf[0] = *(LAS v4u*)(Kp + KCH + 256);
          rd_u(1, 0);
        } else if (cj == 2) lds_wait(S, 9);
        PIN();
#pragma unroll
        for (int R = 0; R < 8; ++R) *(LAS f4*)(Pp0 + (cj & 1) * SLOT + R * 1024) = acc[R];
        lds_put(CNT + 16 * 2 + 4 * wq, cj + 1);
        Kp += KCH; Up += UCH;
      };
#pragma unroll 1
      for (int cj = 0; cj < nch - 1; ++cj) channel(cj, false);
      channel(nch - 1, true);
    }
    STAMP(7);
    __builtin_amdgcn_s_setprio(CPRIO);
    lds_wait_ge(S, CNT + 16 * 3, 1);
    load_h(hb0);
  } else {
    IDS();
    const int wf = w >> 1, tg = w & 1;
    unsigned xa[4], wa[2][2], pn[6];
    unsigned lgn, dskn;
    auto fetch = [&](int bt) {
      const int ch = ch00 + 2 * bt + wf, o = ch * N + tg * 64 + lane;
      pn[0] = Are[o]; pn[1] = Aim[o]; pn[2] = Bre[o]; pn[3] = Bim[o]; pn[4] = Cre[o]; pn[5] = Cim[o];
      lgn = logdt[ch]; dskn = dskip[ch];
      PIN();
    };
    fetch(0);
    int fwk = fw_flag(ct);
    GOT(fwk);
#pragma unroll
    for (int i = 0; i < 4; ++i) {
      const int row = 64 * tg + (lane & 31), c = 2 * wf + (i >> 1), t2 = i & 1;
      xa[i] = (row * 128 + 16 * (h ^ ((row >> 1) & 7))) ^ (32 * i);
      wa[i >> 1][i & 1] = 16384 + 256 * (8 * h + 4 * t2 + q) + 64 * (c ^ q) + 16 * ((2 * blk + (p >> 1)) ^ (2 * h + t2)) + 8 * (p & 1);
      GOT(xa[i]); GOT(wa[i >> 1][i & 1]);
    }
    PIN();
#pragma unroll 1
    for (int bt = 0; bt < nbat; ++bt) {
      const unsigned p0 = pn[0], p1 = pn[1], p2 = pn[2], p3 = pn[3], p4 = pn[4], p5 = pn[5];
      const float lg = bflo(lgn), dsk = bflo(dskn);
      if (bt + 1 < nbat) fetch(bt + 1);
      if (bt) lds_wait_ge(S, CNT + 16 * 5 + 4 * (w ^ 1), bt);
      LAS unsigned* T = (LAS unsigned*)(S + TAB + w * TW) + lane;
      LAS char* Tr = S + TAB + w * TW + g * 16 + (lane & 15) * TROW;
      constexpr int R = TROW / 4;
      const float dt = rbf(__expf(lg)), a = dt * bflo(p0) * 1.4426950408889634f;
      float th = dt * bflo(p1) * 0.15915494309189535f;
      th -= __builtin_rintf(th);
      const f2 e1 = cexpl(a, th, 1.f), e128 = cexpl(a, th, 128.f), e8 = cexpl(a, -th, 8.f);
      const cm m1 = mkm(e1), m128 = mkm(e128), m8 = mkm(e8), m16 = mkm(cmul(e8, m8));
      const f2 e2 = cmul(e1, m1);
      const cm m2 = mkm(e2), m4 = mkm(cmul(e2, m2));
      f2 y = cmul(f2{bflo(p4), -bflo(p5)}, mkm(f2{dt, dt} * f2{bflo(p2), -bflo(p3)})), y2 = cmul(y, m8), t = cexpl(a, th, 1024.f);
#pragma unroll
      for (int i = 0; i < 8; ++i) {
        T[(2 * i) * R] = pk2(y); T[(2 * i + 1) * R] = pk2(y2); T[(16 + i) * R] = pk2(t);
        if (i < 7) { y = cmul(y, m16); y2 = cmul(y2, m16); }
        t = cmul(t, m128);
      }
      T[24 * R] = pk2(t);
      v4u fy[4], fa[4][2];
      f4 ka[3] = {};
#pragma unroll
      for (int ks = 0; ks < 4; ++ks) { fy[ks] = *(LAS v4u*)(Tr + ks * 64); fa[ks][0] = *(LAS v4u*)(Tr + 16 * TROW + ks * 64); }
      PIN();
      f2 u = {1.f, 0.f}, v = e128, z = cmul(e128, m128);
#pragma unroll
      for (int i = 0; i < 8; ++i) {
        T[i * R] = pk2(u); T[(8 + i) * R] = pk2(v);
        if (i < 7) { u = cmul(u, m1); v = cmul(v, m1); }
        if (i < 2) {
          const f2 z2 = cmul(z, m2);
          T[(16 + 4 * i) * R] = pk2(z); T[(17 + 4 * i) * R] = pk2(z2); T[(18 + 4 * i) * R] = pk2(cmul(z, m4)); T[(19 + 4 * i) * R] = pk2(cmul(z2, m4));
        } else if (i < 6) {
          T[(20 + 2 * i) * R] = pk2(z); T[(21 + 2 * i) * R] = pk2(cmul(z, m4));
        }
        if (i < 5) z = cmul(z, m128);
      }
      PIN();
#pragma unroll
      for (int ks = 0; ks < 4; ++ks) ka[2] = __builtin_amdgcn_mfma_f32_16x16x32_bf16(asbf(fa[ks][0]), asbf(fy[ks]), ka[2], 0, 0, 0);
#pragma unroll
      for (int ks = 0; ks < 4; ++ks) { fa[ks][0] = *(LAS v4u*)(Tr + ks * 64); fa[ks][1] = *(LAS v4u*)(Tr + 16 * TROW + ks * 64); }
      PIN();
#pragma unroll
      for (int ks = 0; ks < 4; ++ks)
#pragma unroll
        for (int i = 0; i < 2; ++i) ka[i] = __builtin_amdgcn_mfma_f32_16x16x32_bf16(asbf(fa[ks][i]), asbf(fy[ks]), ka[i], 0, 0, 0);
      if (bt == 0) STAMP(1);
      {
        LAS char* X = S + TAB + w * TW + 4608 + lane * 16;
        *(LAS f4*)X = tg ? ka[0] : ka[1];
        *(LAS f4*)(X + 1024) = ka[2];
        lds_put(CNT + 16 * 4 + 4 * w, bt + 1);
        lds_wait_ge(S, CNT + 16 * 4 + 4 * (w ^ 1), bt + 1);
        LAS char* Y = S + TAB + (w ^ 1) * TW + 4608 + lane * 16;
        f4 km = (tg ? ka[1] : ka[0]) + *(LAS f4*)Y, k2 = ka[2] + *(LAS f4*)(Y + 1024);
        GOT(km); GOT(k2);
        lds_put(CNT + 16 * 5 + 4 * w, bt + 1);
        const int c = lane & 15;
        LAS char* K = S + kar + (2 * bt + wf) * KCH;
        LAS u16* T1 = (LAS u16*)(S + TAB + w * TW);
        const unsigned q01 = pk2(k2.xy), q23 = pk2(k2.zw);
        if (!tg) {
          if (lane == 0) km.x += dsk;
          *(LAS v2u*)(K + 512 + 256 * (g >> 1) + 16 * c + 8 * blk) = v2u{pk2(km.xy), pk2(km.zw)};
          LAS u16* T3 = T1 + 64 * g + c;
          T3[0] = (u16)q01; T3[16] = (u16)(q01 >> 16); T3[32] = (u16)q23; T3[48] = (u16)(q23 >> 16);
          ORDER();
          const unsigned c0_ = T1[lane], c1 = T1[1 + lane], d0 = T1[64 + lane], d1 = T1[65 + lane];
          if (h) *(LAS v4u*)(K + 16 * (lane & 31)) = v4u{0u, 0u, 0u, 0u};
          *(LAS v4u*)(K + 2560 + 16 * lane) = v4u{c0_ * 0x10001u, c0_ * 0x10001u, ((lane & 1) ? c1 : c0_) | (c1 << 16), c1 * 0x10001u};
          *(LAS v4u*)(K + 3584 + 16 * lane) = v4u{d0 * 0x10001u, d0 * 0x10001u, ((lane & 1) ? d1 : d0) | (d1 << 16), d1 * 0x10001u};
        } else {
          const unsigned p01 = pk2(km.xy), p23 = pk2(km.zw);
          if (g < 2) *(LAS v2u*)(T1 + 64 * g + 4 * c) = v2u{p01, p23};
          else { *(LAS unsigned*)(T1 + 64 * g + 2 * c) = p01; *(LAS unsigned*)(T1 + 64 * g + 32 + 2 * c) = p23; }
          if (lane == 0) T1[256] = (u16)q01;
          ORDER();
          const v2u a1 = *(LAS v2u*)(T1 + 4 * (lane & 31));
          const unsigned a4 = T1[4 * (lane & 31) + 4], b0 = *(LAS unsigned*)(T1 + 128 + 2 * lane), b2_ = T1[130 + 2 * lane];
          if (!h) *(LAS v4u*)(K + 1024 + 16 * lane) = v4u{(a1.x & 0xffffu) * 0x10001u, (a1.x >> 16) | (a1.y << 16), (a1.y & 0xffffu) * 0x10001u, (a1.y >> 16) | (a4 << 16)};
          *(LAS v4u*)(K + 1536 + 16 * lane) = v4u{(b0 & 0xffffu) * 0x10001u, b0, (b0 >> 16) * 0x10001u, b2_ * 0x10001u};
        }
      }
    }
    if (cb < 10 && !spare) {
      lds_wait(S, 1);
      v2u xt[8];
#pragma unroll
      for (int m = 0; m < 8; ++m) {
        const int rr = 32 * g + 4 * m + q;
        xt[m] = trd(S + 128 * rr + 16 * ((2 * w + (p >> 1)) ^ ((rr >> 1) & 7)) + 8 * (p & 1));
      }
      LAS char* X = S + TAB + w * TW;
#pragma unroll
      for (int k = 0; k < 4; ++k) *(LAS v4u*)(X + (lane & 15) * 272 + g * 64 + k * 16) = v4u{xt[2 * k].x, xt[2 * k].y, xt[2 * k + 1].x, xt[2 * k + 1].y};
#pragma unroll
      for (int k = 0; k < 4; ++k)
        st16(rws, *(LAS v4u*)(X + (4 * k + g) * 272 + (lane & 15) * 16), WS_XT + (s0 * 64 + 16 * w + 4 * k + g) * 4096 + rp * 256 + (lane & 15) * 16);
    }
    STAMP(2);
    if (!spare) {
      f16v acc[2][2] = {};
      v4u fx[4][2], fw[4][2];
      auto rd = [&](int set, int so, int ks) {
        fw[set][0] = trd2(S + wa[0][0] + so + 4096 * ks, S + wa[0][1] + so + 4096 * ks);
        fx[set][0] = *(LAS v4u*)(S + xa[ks] + so);
        fx[set][1] = *(LAS v4u*)(S + xa[ks] + so + 4096);
        fw[set][1] = trd2(S + wa[1][0] + so + 4096 * ks, S + wa[1][1] + so + 4096 * ks);
      };
      auto mm = [&](int set) {
#pragma unroll
        for (int i = 0; i < 4; ++i) acc[i >> 1][i & 1] = __builtin_amdgcn_mfma_f32_32x32x16_bf16(asbf(fw[set][i >> 1]), asbf(fx[set][i & 1]), acc[i >> 1][i & 1], 0, 0, 0);
#pragma unroll
        for (int i = 0; i < 3; ++i) { __builtin_amdgcn_sched_group_barrier(0x8, 1, 0); __builtin_amdgcn_sched_group_barrier(0x100, 2, 0); }
        __builtin_amdgcn_sched_group_barrier(0x8, 1, 0);
        PIN();
      };
      u64 fw_seen = 0, g0 = 0, g1 = 0, gc = 0;
      unsigned b1v = 0;
      const unsigned o0 = WS_ST + (lane & 7) * 2048 + (rp * 16 + 8 * tg + ((lane & 31) >> 3)) * 8, oc = WS_CS + (c0 + 64 * wf + lane) * 8;
      asm volatile("" : "+v"(acc[0][0]), "+v"(acc[0][1]), "+v"(acc[1][0]), "+v"(acc[1][1]));
      bar();
      STAMP(6);
      tag = rd_tag(S);
      rd(0, 0, 0); rd(1, 0, 1);
      PIN();
      auto flip = [&]() {
#pragma unroll
        for (int i = 0; i < 4; ++i) { xa[i] ^= 2 * SLOT; wa[i >> 1][i & 1] ^= 2 * SLOT; }
      };
#pragma unroll 1
      for (int n = 0; n < 4; ++n) {
        if (n == 3) { fw_seen = ld_hidden(ws, WS_FLAG + 128u * fwk); g0 = ld_hidden(ws, o0); g1 = ld_hidden(ws, o0 + 32); gc = ld_hidden(ws, oc);
          asm volatile("global_load_ushort %0, %1, %2" : "=v"(b1v) : "v"((c0 + 64 * wf + lane) * 2), "s"(b1) : "memory"); }
        rd(2, 0, 2); mm(0);
        rd(3, 0, 3); mm(1);
        rd(0, SLOT, 0); mm(2);
        rd(1, SLOT, 1); mm(3);
        if (n == FXN) WAITV(0);
        bar_raw();
        if (n == FXN && w == 1 && cb < 10 && lane == 0) put_flag(ws, FL_X + rp * 10 + (SKEW == 1 ? s0 : cb), tag);
        rd(2, SLOT, 2); mm(0);
        rd(3, SLOT, 3); mm(1);
        flip();
        rd(0, 0, 0); mm(2);
        rd(1, 0, 1); mm(3);
        bar_raw();
      }
      WAITV(0); GOT(fw_seen); GOT(g0); GOT(g1); GOT(gc); GOT(b1v);
      if (!tag_ok(gc, tag)) gc = wait_granule(ws, oc, tag);
      *(LAS unsigned*)(S + COLT + w * 512 + 256 + lane * 4) = b1v << 16;
      *(LAS unsigned*)(S + COLT + w * 512 + lane * 4) = (unsigned)gc;
      if (!tag_ok(g0, tag)) g0 = wait_granule(ws, o0, tag);
      if (!tag_ok(g1, tag)) g1 = wait_granule(ws, o0 + 32, tag);
      check_flags(ws, fwk, fw_seen, tag);
      PIN();
      rd(2, 0, 2); mm(0);
      rd(3, 0, 3); mm(1);
      rd(0, SLOT, 0); mm(2);
      rd(1, SLOT, 1); mm(3);
      rd(2, SLOT, 2); mm(0);
      rd(3, SLOT, 3); mm(1);
      mm(2);
      mm(3);
      STAMP(3);
      const unsigned hv = WS_HT + (((rp * 8 + 4 * tg + blk) * 54 + (c0 >> 5) + 2 * wf) * 64 + (lane & 15) + 16 * h) * 16 + ((cb == 13 && wf == 0) ? OOB : 0u);
#pragma unroll
      for (int c = 0; c < 2; ++c) {
        f4 cs[4], bb[4];
#pragma unroll
        for (int a = 0; a < 4; ++a) { LAS char* ct_ = S + COLT + w * 512 + (32 * c + 8 * a + 4 * h) * 4; cs[a] = *(LAS f4*)ct_; bb[a] = *(LAS f4*)(ct_ + 256); }
#pragma unroll
        for (int tt = 0; tt < 2; ++tt) {
          const float rs = __builtin_amdgcn_rcpf(bfhi((unsigned)(tt ? g1 : g0))), mr = -bflo((unsigned)(tt ? g1 : g0)) * rs;
          unsigned o[8];
#pragma unroll
          for (int a = 0; a < 4; ++a) {
#pragma unroll
            for (int e = 0; e < 2; ++e) {
              const f2 v = fma2(f2{acc[c][tt][4 * a + 2 * e], acc[c][tt][4 * a + 2 * e + 1]}, f2{rs, rs}, fma2(f2{mr, mr}, e ? cs[a].zw : cs[a].xy, e ? bb[a].zw : bb[a].xy));
              const f2 z = v * fma2(v * v, f2{-0.10294324f, -0.10294324f}, f2{-2.30220819f, -2.30220819f});
              const f2 dn = f2{__builtin_amdgcn_exp2f(z.x), __builtin_amdgcn_exp2f(z.y)} + f2{1.f, 1.f};
              o[2 * a + e] = pk2(v * f2{__builtin_amdgcn_rcpf(dn.x), __builtin_amdgcn_rcpf(dn.y)});
            }
          }
          load_w2_(w2s, NA * (2 * c + tt), NA * (2 * c + tt + 1));
          st16(rws, v4u{o[0], o[1], o[2], o[3]}, hv + (tt * 108 + c) * 1024);
          st16(rws, v4u{o[4], o[5], o[6], o[7]}, hv + (tt * 108 + c) * 1024 + 512);
          PIN();
        }
      }
      STAMP(4);
      lds_put(CNT + 16 * 6 + 4 * wq, 1);
      load_w2_(w2s, 4 * NA, 4 * NA + 8);
      WAITV(8);
      if (lds_arrive(S, 0, wq) && lane == 0) put_flag(ws, FL_H + gi, tag);
      STAMP(5);
      load_w2_(w2s, 4 * NA + 8, 35);
      auto publish = [&](int j) {
        lds_wait_ge(S, CNT + 16 * 2 + 4 * p, j + 1);
        const unsigned pa = (j & 1) * SLOT + (tid >> 5) * 1024 + (((tid >> 1) & 15) ^ (4 * (tid & 1))) * 16 + (tid & 1) * 512;
        f4 lo[4], hi[4], l, m;
#pragma unroll
        for (int v = 0; v < 4; ++v) { lo[v] = *(LAS f4*)(S + pa + v * 8192); hi[v] = *(LAS f4*)(S + (pa ^ 32u) + 256 + v * 8192); }
#pragma unroll
        for (int e = 0; e < 4; ++e) { l[e] = sadd(sadd(lo[0][e], lo[1][e]), sadd(lo[2][e], lo[3][e])); m[e] = sadd(sadd(hi[0][e], hi[1][e]), sadd(hi[2][e], hi[3][e])); }
        GOT(l); GOT(m);
        lds_put(CNT + 16 * 9 + 4 * wq, 1);
        st16(rws, v4u{pk2(l.xy), pk2(l.zw), pk2(m.xy), pk2(m.zw)}, tid * 16 + WS_YT + (j < 2 ? ch00 + j : ch3) * 4096);
      };
      publish(0);
      wait_flags(ws, fhk, tag);
      if (w == 0) lds_put(CNT + 16 * 3, 1);
      load_h(hb0 + 4096);
#pragma unroll
      for (int i = 0; i < 7; ++i) GOT(hf[i]);
      load_h(hb0);
      PIN();
#pragma unroll 1
      for (int j = 1; j < nch; ++j) publish(j);
    } else {
      lds_arrive(S, 6, wq);
      lds_wait(S, 6);
      tag = get_tag(S);
#pragma unroll
      for (int j = 0; j < 6; ++j) st16(rws, *(LAS v4u*)(S + KARR + j * KCH + 512 + tid * 16), tid * 16 + WS_K3 + (ch00 + j) * 4096);
      WAITV(0);
      if (lds_arrive(S, 0, wq) && lane == 0) put_flag(ws, FL_K + sp, tag);
      if (MAP48) { warm_w2(2 * (bi >> 3), tag); if ((bi >> 3) != 3) warm_w2(2 * (bi >> 3) + 1, tag); } else if ((bi >> 3) != 3) warm_w2(4 * (xcd & 1) + (bi >> 3), tag);
      wait_flags(ws, fwk, tag);
      load_w2_(w2s, 0, 35);
      wait_flags(ws, fhk, tag);
      if (w == 0) lds_put(CNT + 16 * 3, 1);
      load_h(hb0);
    }
    STAMP(7);
  }
  {
    IDS();
    STAMP(8);
    unsigned hb[4];
#pragma unroll
    for (int pt = 0; pt < 4; ++pt) hb[pt] = WS_HT + ((rt * 4 + ((pt + ct) & 3)) * 54 + w) * 1024;
    STAMP(9);
    LAS char* Pw = S + w * GW + g * 256 + (((lane & 15) + 4 * g) & 15) * 16;
    const __amdgpu_buffer_rsrc_t rx = __builtin_amdgcn_make_buffer_rsrc((void*)x, 0, L * D * 2, 0x00020000),
                                 ro = __builtin_amdgcn_make_buffer_rsrc(out, 0, L * D * 2, 0x00020000),
                                 rb2 = __builtin_amdgcn_make_buffer_rsrc((void*)b2, 0, D * 2, 0x00020000);
    const int fyk = FL_Y + ct * 28 + (lane < 28 ? lane : 0);
    const unsigned tl_p = p * 256 + (((lane >> 2) + 4 * p) & 15) * 16, tl_o = ((lane >> 2) * D + 4 * p) * 2, tl_y = YT2 + (lane >> 2) * YT2ROW + 8 * p;
    unsigned tpr[3], tov[3], tyr[3];
    v2u xr[3], bb[3];
    f4 p0[4], s0a;
    v4u yv[2];
    auto task_setup = [&](int i) {
      const int id = i == 0 ? w : i == 1 ? w + 8 : w + 15, pt = id / 5, n = id % 5, t0 = ((pt + ct) & 3) * 16;
      const bool on = i == 0 || (i == 1 ? w < 7 : w < 5);
      tpr[i] = tl_p + (pt >= 3 ? 0 : pt) * GBUF + n * GT;
      tov[i] = tl_o + ((rt * 64 + t0) * D + ct * 80 + 16 * n) * 2 + (on ? 0u : OOB);
      tyr[i] = tl_y + t0 * YT2ROW + 32 * n;
      xr[i] = __builtin_amdgcn_raw_buffer_load_b64(rx, tov[i], 0, 0);
      bb[i] = __builtin_amdgcn_raw_buffer_load_b64(rb2, p * 8, (ct * 80 + 16 * n) * 2, 0);
    };
    auto bias = [&](int i, f4 sa) { return v2u{pk2(unpk(pk2(sa.xy)) + unpk(bb[i].x)), pk2(unpk(pk2(sa.zw)) + unpk(bb[i].y))}; };
    auto reduce = [&](int i) {
      f4 pr[8];
#pragma unroll
      for (int v = 0; v < 8; ++v) pr[v] = *(LAS f4*)(S + tpr[i] + v * GW);
      const f4 sa = ((pr[0] + pr[1]) + (pr[2] + pr[3])) + ((pr[4] + pr[5]) + (pr[6] + pr[7]));
      return bias(i, sa);
    };
    auto finish = [&](int i, v2u yb) {
      const v2u y = *(LAS v2u*)(S + tyr[i]);
      __builtin_amdgcn_raw_buffer_store_b64(v2u{pk2(unpk(pk2(unpk(xr[i].x) + unpk(y.x))) + unpk(yb.x)), pk2(unpk(pk2(unpk(xr[i].y) + unpk(y.y))) + unpk(yb.y))}, ro, tov[i], 0, 0);
    };
    f4 acc[2][5];
#pragma unroll
    for (int pt = 0; pt < 4; ++pt) {
#pragma unroll
      for (int j = 0; j < 7; ++j) {
        if (pt == 3 && j == 5) {
#pragma unroll
          for (int k = 0; k < 2; ++k)
            if (k == 0 || w < 2) {
              const int f = w + 8 * k;
#pragma unroll
              for (int hf_ = 0; hf_ < 2; ++hf_) {
                const unsigned e = (lane & 15) + 16 * hf_ - 8 * g, one = e < 8u ? 0x3f80u << (16 * (e & 1)) : 0u;
                const v4u sel = {(e >> 1) == 0 ? one : 0u, (e >> 1) == 1 ? one : 0u, (e >> 1) == 2 ? one : 0u, (e >> 1) == 3 ? one : 0u};
                const f4 yt = __builtin_amdgcn_mfma_f32_16x16x32_bf16(asbf(yv[k]), asbf(sel), f4{0.f, 0.f, 0.f, 0.f}, 0, 0, 0);
                *(LAS v2u*)(S + YT2 + (32 * (f / 5) + 16 * hf_ + (lane & 15)) * YT2ROW + 32 * (f % 5) + 8 * g) = v2u{pk2(yt.xy), pk2(yt.zw)};
              }
            }
        }
        if (pt == 3) {
          auto add4 = [](f4 a, f4 c) { return f4{sadd(a.x, c.x), sadd(a.y, c.y), sadd(a.z, c.z), sadd(a.w, c.w)}; };
          if (j == 2) { s0a = add4(add4(p0[0], p0[1]), add4(p0[2], p0[3])); GOT(s0a); }
          if (j == 4) { s0a = add4(s0a, add4(add4(p0[0], p0[1]), add4(p0[2], p0[3]))); GOT(s0a); lds_put(CNT + 16 * (11 + (w >> 2)) + 4 * wq, 1); }
          if (j == 0 || j == 2) {
#pragma unroll
            for (int v = 0; v < 4; ++v) p0[v] = *(LAS f4*)(S + tpr[0] + (2 * j + v) * GW);
          }
          if (j == 3) { task_setup(1); task_setup(2); }
        }
        if (j >= 1 && j < 6 && pt >= 1) *(LAS f4*)(Pw + (pt - 1) * GBUF + (j - 1) * GT) = acc[(pt - 1) & 1][j - 1];
#pragma unroll
        for (int n = 0; n < 5; ++n)
          acc[pt & 1][n] = __builtin_amdgcn_mfma_f32_16x16x32_bf16(asbf(wf2[5 * j + n]), asbf(hf[j]), j ? acc[pt & 1][n] : f4{0.f, 0.f, 0.f, 0.f}, 0, 0, 0);
        if (pt < 3) hf[j] = ld16(rws, w2v, hb[pt + 1] + (j < 6 ? j * 8192u : k6));
        PIN();
        if (pt == 0 && j == FYJ && w < 4 && !spare) {
          WAITV(7);
          if (lds_arrive(S, 10, wq) && lane == 0) put_flag(ws, FL_Y + gi, tag);
        }
        if (pt == 1 && j == 3) fh = ld_hidden(ws, WS_FLAG + 128u * fyk);
        if (pt == 2 && j == 3) {
          WAITV(7);
          GOT(fh);
          check_flags(ws, fyk, fh, tag);
#pragma unroll
          for (int k = 0; k < 2; ++k) {
            const int f = w + 8 * k;
            if (k == 0 || w < 2) yv[k] = ld16(rws, (lane & 15) * 4096 + g * 16, WS_YT + (ct * 80 + 16 * (f % 5)) * 4096 + rt * 128 + 64 * (f / 5));
          }
          PIN();
        }
      }
      if (pt == 0) { STAMP(11); bar(); }
      if (pt == 1) STAMP(14);
      if (pt == 1) { task_setup(0); PIN(); }
      if (pt == 2) { if (w < 4) STAMP(2); bar(); }
    }
    if (w < 4) STAMP(12);
    lds_wait_ge(S, CNT + 16 * 11 + 4 * (lane & 7), 1);
#pragma unroll
    for (int n = 0; n < 5; ++n) *(LAS f4*)(Pw + n * GT) = acc[1][n];
    bar();
    if (w < 4) STAMP(13);
    finish(0, bias(0, s0a));
    const v2u y1 = reduce(1), y2 = reduce(2);
    finish(1, y1);
    finish(2, y2);
  }
  STAMP(10);
  STAMP_OUT();
  if (b == 0 && w == 7) {
#pragma unroll 1
    for (unsigned s = 0, j = 0; s < SPIN_CAP && j < 4; ++s)
      if (__all((unsigned)(ld_word(ws, WS_ST + (64 * j + lane) * 8) >> 32) == (unsigned)tag)) ++j;
    if (lane == 0) __hip_atomic_store((GAS u64*)ws, tag, __ATOMIC_RELAXED, __HIP_MEMORY_SCOPE_AGENT);
  }
  asm volatile("s4_code_end:");
}
}

extern "C" void kernel_launch(void* const* d_in, const int* in_sizes, int n_in, void* d_out, int out_size, void* d_ws, size_t ws_size,
                              hipStream_t stream) {
  (void)in_sizes; (void)n_in; (void)out_size; (void)ws_size;
  (void)hipFuncSetAttribute((const void*)s4mlp_kernel, hipFuncAttributeMaxDynamicSharedMemorySize, LDS_BYTES);
  s4mlp_kernel<<<256, 512, LDS_BYTES, stream>>>((const u16*)d_in[0], (const u16*)d_in[1], (const u16*)d_in[2], (const u16*)d_in[3], (const u16*)d_in[4],
                                                (const u16*)d_in[5], (const u16*)d_in[6], (const u16*)d_in[11], (const u16*)d_in[7], (const u16*)d_in[8],
                                                (const u16*)d_in[10], (const u16*)d_in[12], (const u16*)d_in[13], (u16*)d_out, (char*)d_ws);
}
```

```cpp
#include <hip/hip_runtime.h>
#include <hip/hip_bf16.h>
#include <stdint.h>

namespace {
typedef unsigned short u16;
typedef unsigned long long u64;
typedef __bf16 bf8 __attribute__((ext_vector_type(8)));
typedef __bf16 bf2 __attribute__((ext_vector_type(2)));
typedef short s4 __attribute__((ext_vector_type(4)));
typedef float f16v __attribute__((ext_vector_type(16)));
typedef float f4 __attribute__((ext_vector_type(4)));
typedef float f2 __attribute__((ext_vector_type(2)));
typedef unsigned v4u __attribute__((ext_vector_type(4)));
typedef unsigned v2u __attribute__((ext_vector_type(2)));
#define LAS __attribute__((address_space(3)))
#define GAS __attribute__((address_space(1)))
#define DI __device__ __forceinline__

constexpr int L = 2048, D = 640, N = 128, F = 1728;
constexpr unsigned WS_END0 = (1u << 20) + 168u * 54 * 1024 + 2 * 640u * 4096;
constexpr unsigned WS_FLAG = 4096;
constexpr int FL_H = 0, FL_X = 224, FL_Y = 384, FL_K = 608, FL_W = 640;
constexpr unsigned WS_ST = 256u << 10;
constexpr unsigned WS_CS = WS_ST + 2048 * 8;
constexpr unsigned WS_K3 = WS_END0;
constexpr unsigned WS_HT = 1u << 20;
constexpr unsigned WS_W2T = WS_HT + 128u * 54 * 1024;
constexpr unsigned WS_XT = WS_W2T + 40u * 54 * 1024;
constexpr unsigned WS_YT = WS_XT + 640u * 4096;
constexpr unsigned WS_END = WS_YT + 2 * 640u * 4096;
static_assert(WS_K3 == WS_YT + 640u * 4096, "workspace map");
constexpr unsigned WS_DBG = 32u << 20;
constexpr unsigned OOB = 0x40000000u;
constexpr int LDS_BYTES = 163840;
constexpr int SLOT = 32768;
constexpr int TAB = 98304, TROW = 272, TW = 32 * TROW;
constexpr int CNT = TAB + 4 * TW, TAGL = CNT + 240;
constexpr int COLT = CNT + 256;
constexpr int W2U_G = COLT + 2048, W2U_S = 16384;
constexpr int KCH = 4608, KARR = LDS_BYTES - 6 * KCH;
constexpr int UCP = 65536, UST = 4160, UCH = 4 * UST;
constexpr int GT = 1024, GW = 5 * GT, GBUF = 8 * GW;
static_assert(W2U_G + 4096 <= KARR + 3 * KCH && UCP + 3 * UCH <= CNT && 3 * GBUF <= CNT, "LDS map");
#define NB1 20
#define NB0 8
#define WARM 0
#define NA 0
#define CPRIO 0
#define FYJ 6
#define FXN 1
#define BPRIO 1
#define SKEW 1
#define MAP48 1
#define WARMA 1
#define WARMB 2
#define LAUX 16
constexpr unsigned SPIN_CAP = 1u << 17;
#define SLP 1

DI float bflo(unsigned v) { return __uint_as_float(v << 16); }
DI float bfhi(unsigned v) { return __uint_as_float(v & 0xffff0000u); }
DI f2 unpk(unsigned v) { return f2{bflo(v), bfhi(v)}; }
DI unsigned pk2(f2 p) { return __builtin_bit_cast(unsigned, __builtin_convertvector(p, bf2)); }
DI unsigned pk(float a, float b) { return pk2(f2{a, b}); }
DI float rbf(float a) { return bflo(pk(a, a)); }
DI bf8 asbf(v4u v) { return __builtin_bit_cast(bf8, v); }
DI v4u mk_srd(const void* p, unsigned bytes) { u64 a = (u64)p; return v4u{(unsigned)a, (unsigned)(a >> 32) & 0xffffu, bytes, 0x00020000u}; }
#define SET_M0(v) asm volatile("s_mov_b32 m0, %0\n\ts_nop 0" ::"s"(__builtin_amdgcn_readfirstlane(v)) : "memory")
#define DMA(srd, voff, soff, imm) asm volatile("buffer_load_dwordx4 %0, %1, %2 offen offset:" #imm " lds" ::"v"(voff), "s"(srd), "s"(soff) : "memory")
#define WAITV(n) asm volatile("s_waitcnt vmcnt(" #n ")" ::: "memory")
#define PIN() __builtin_amdgcn_sched_barrier(0)
#define ORDER() asm volatile("" ::: "memory")
DI void bar() { asm volatile("s_waitcnt lgkmcnt(0)\n\ts_barrier" ::: "memory"); }
DI void bar_raw() { PIN(); asm volatile("s_barrier" ::: "memory"); PIN(); }
DI v2u trd(LAS char* p) { return __builtin_bit_cast(v2u, __builtin_amdgcn_ds_read_tr16_b64_v4i16((LAS s4*)p)); }
DI v4u trd2(LAS char* p0, LAS char* p1) { v2u a = trd(p0), b = trd(p1); return v4u{a.x, a.y, b.x, b.y}; }
DI float dot2(unsigned a, unsigned b, float c) { return __builtin_amdgcn_fdot2_f32_bf16(__builtin_bit_cast(bf2, a), __builtin_bit_cast(bf2, b), c, false); }
DI float dot8(v4u a, v4u b, float c) { return dot2(a.w, b.w, dot2(a.z, b.z, dot2(a.y, b.y, dot2(a.x, b.x, c)))); }
DI float sum8(v4u a, float c) { const unsigned o = 0x3f803f80u; return dot2(a.w, o, dot2(a.z, o, dot2(a.y, o, dot2(a.x, o, c)))); }
DI f2 fma2(f2 a, f2 b, f2 c) { return __builtin_elementwise_fma(a, b, c); }
struct cm { f2 p, n; };
DI cm mkm(f2 b) { return cm{b, f2{-b.y, b.x}}; }
DI f2 cmul(f2 a, cm b) { return fma2(a.yy, b.n, a.xx * b.p); }
DI f2 cexpl(float a, float th, float l) {
  const float m = __builtin_amdgcn_exp2f(a * l), rv = __builtin_amdgcn_fractf(th * l);
  return f2{m, m} * f2{__builtin_amdgcn_cosf(rv), __builtin_amdgcn_sinf(rv)};
}
DI float xor32(float v) { const auto r = __builtin_amdgcn_permlane32_swap(__float_as_uint(v), __float_as_uint(v), false, false); return __uint_as_float(r[0]) + __uint_as_float(r[1]); }
DI void st16(__amdgpu_buffer_rsrc_t r, v4u v, unsigned off) { __builtin_amdgcn_raw_buffer_store_b128(v, r, off, 0, 16); }
DI v4u ld16(__amdgpu_buffer_rsrc_t r, unsigned voff, unsigned soff) { return __builtin_amdgcn_raw_buffer_load_b128(r, voff, soff, LAUX); }
DI void put_flag(char* ws, int k, u64 tag) { __hip_atomic_store((GAS u64*)(ws + WS_FLAG + 128u * k), tag, __ATOMIC_RELAXED, __HIP_MEMORY_SCOPE_AGENT); }
DI u64 ld_word(char* ws, unsigned off) { return __hip_atomic_load((GAS u64*)(ws + off), __ATOMIC_RELAXED, __HIP_MEMORY_SCOPE_AGENT); }
DI void wait_flags(char* ws, int k, u64 tag) {
#pragma unroll 1
  for (unsigned s = 0; s < SPIN_CAP; ++s) {
    if (__all(ld_word(ws, WS_FLAG + 128u * k) == tag)) break;
    __builtin_amdgcn_s_sleep(1);
  }
  __builtin_amdgcn_fence(__ATOMIC_ACQUIRE, "wavefront");
}
DI u64 next_tag(u64 ep) { return ep * 6364136223846793005ull + 1442695040888963407ull; }
DI float ld_bf_uniform(const u16* p, int i) { const unsigned v = ((const unsigned*)p)[i >> 1]; return (i & 1) ? bfhi(v) : bflo(v); }
DI void lds_put(unsigned addr, unsigned v) { asm volatile("s_mov_b64 exec, 1\n\tds_write_b32 %0, %1\n\ts_mov_b64 exec, -1" ::"v"(addr), "v"(v) : "memory"); }
DI bool lds_arrive(LAS char* S, int c, int q) {
  asm volatile("s_waitcnt lgkmcnt(0)" ::: "memory");
  lds_put(CNT + 16 * c + 4 * q, 1u);
  const v4u v = *(volatile LAS v4u*)(S + CNT + 16 * c);
  return __builtin_amdgcn_readfirstlane(v.x & v.y & v.z & v.w);
}
DI void lds_wait(LAS char* S, int c) {
#pragma unroll 1
  for (unsigned s = 0; s < (SPIN_CAP << 4); ++s) {
    const v4u v = *(volatile LAS v4u*)(S + CNT + 16 * c);
    if (__builtin_amdgcn_readfirstlane(v.x & v.y & v.z & v.w)) break;
    __builtin_amdgcn_s_sleep(SLP);
  }
  ORDER();
}
DI void lds_set(LAS char* S, int off, unsigned v) { asm volatile("s_waitcnt lgkmcnt(0)" ::: "memory"); lds_put(off, v); }
DI unsigned lds_get(LAS char* S, unsigned off) { return *(volatile LAS unsigned*)(S + off); }
DI void lds_wait_ge(LAS char* S, unsigned off, unsigned n) {
#pragma unroll 1
  for (unsigned s = 0; s < (SPIN_CAP << 4); ++s) {
    if (__all(lds_get(S, off) >= n)) break;
    __builtin_amdgcn_s_sleep(SLP);
  }
  ORDER();
}
DI u64 rd_tag(LAS char* S) { const v2u t = *(volatile LAS v2u*)(S + TAGL); return ((u64)(unsigned)__builtin_amdgcn_readfirstlane(t.y) << 32) | (unsigned)__builtin_amdgcn_readfirstlane(t.x); }
DI u64 get_tag(LAS char* S) { lds_wait_ge(S, TAGL + 8, 1); return rd_tag(S); }
DI u64 ld_hidden(char* ws, unsigned off) { u64 v; asm volatile("global_load_dwordx2 %0, %1, %2 sc1" : "=v"(v) : "v"(off), "s"(ws) : "memory"); return v; }
DI float sadd(float a, float b) { float r; asm("v_add_f32 %0, %1, %2" : "=v"(r) : "v"(a), "v"(b)); return r; }
DI float smul(float a, float b) { float r; asm("v_mul_f32 %0, %1, %2" : "=v"(r) : "v"(a), "v"(b)); return r; }
DI float sfms(float a, float b, float c) { float r; asm("v_fma_f32 %0, %1, %2, -%3" : "=v"(r) : "v"(a), "v"(b), "v"(c)); return r; }
DI float ssub(float a, float b) { float r; asm("v_sub_f32 %0, %1, %2" : "=v"(r) : "v"(a), "v"(b)); return r; }
DI bool tag_ok(u64 g, u64 tag) { return __all((unsigned)(g >> 32) == (unsigned)tag); }
DI u64 wait_granule(char* ws, unsigned off, u64 tag) {
  u64 g = 0;
#pragma unroll 1
  for (unsigned s = 0; s < SPIN_CAP; ++s) { g = ld_word(ws, off); if (tag_ok(g, tag)) break; }
  return g;
}
#define GOT(v) asm volatile("" : "+v"(v))
DI void check_flags(char* ws, int k, u64 seen, u64 tag) {
  if (!__all(seen == tag)) wait_flags(ws, k, tag);
  __builtin_amdgcn_fence(__ATOMIC_ACQUIRE, "wavefront");
}
#define STAMP(k)
#define STAMP_DECL
#define STAMP_OUT()

__global__ __launch_bounds__(512, 2) void s4mlp_kernel(const u16* __restrict__ x, const u16* __restrict__ Are, const u16* __restrict__ Aim,
                                                      const u16* __restrict__ Bre, const u16* __restrict__ Bim, const u16* __restrict__ Cre,
                                                      const u16* __restrict__ Cim, const u16* __restrict__ b1, const u16* __restrict__ logdt,
                                                      const u16* __restrict__ dskip, const u16* __restrict__ w1, const u16* __restrict__ w2,
                                                      const u16* __restrict__ b2, u16* __restrict__ out, char* ws) {
  LAS char* const S = (LAS char*)0;
  const int tid = threadIdx.x, lane = tid & 63, w = __builtin_amdgcn_readfirstlane(tid >> 6), wq = w & 3;
  const int b = blockIdx.x;
  STAMP_DECL;
  STAMP(0);
  v4u pf0 = {}, pf1 = {};
  if (w == 7) {
    unsigned len;
    asm volatile("s4_code_begin:\n\ts_mov_b32 %0, s4_code_end-s4_code_begin-256" : "=s"(len));
    const v4u sc = mk_srd((const void*)__builtin_amdgcn_s_getpc(), __builtin_amdgcn_readfirstlane(len));
    asm volatile("s_nop 4\n\tbuffer_load_dwordx4 %0, %2, %3, 0 offen\n\tbuffer_load_dwordx4 %1, %4, %3, 0 offen"
                 : "=&v"(pf0), "=&v"(pf1) : "v"((b >> 3) * 1024 + lane * 16), "s"(sc), "v"((b >> 3) * 1024 + lane * 16 + 32768) : "memory");
    *(LAS unsigned*)(S + CNT + 4 * lane) = 0u;
  }
  asm volatile("" ::"s"(x), "s"(w1), "s"(ws), "s"(Are), "s"(Aim), "s"(Bre), "s"(Bim), "s"(Cre), "s"(Cim), "s"(w2), "s"(logdt), "s"(dskip));
  bar();
#define IDS()                                                                                                              \
  int bl = b;                                                                                                              \
  asm volatile("" : "+s"(bl));                                                                                             \
  bl = __builtin_amdgcn_readfirstlane(bl);                                                                                 \
  const int xcd = bl & 7, bi = bl >> 3;                                                                                    \
  const int rp = 4 * (xcd >> 1) + (bi >> 3), cb = 7 * (xcd & 1) + (bi & 7);                                 \
  const bool spare = (bi & 7) == 7;                                                                       \
  const int c0 = cb * 128 > F - 128 ? F - 128 : cb * 128, gi = rp * 14 + cb, sp = xcd * 4 + (bi >> 3);                      \
  const int gq = gi / 28, gm = gi % 28;   \
  const int ch00 = spare ? 80 * (sp >> 2) + 56 + 6 * (sp & 3) : 80 * gq + 2 * gm, ch3 = 80 * gq + 56 + gm;                   \
  const int nbat = spare ? 3 : 1, nch = gm < 24 ? 3 : 2;                                                                   \
  const int rt = MAP48 ? 4 * xcd + (bi >> 3) : 8 * (xcd >> 1) + (bi >> 2), ct = MAP48 ? (bi & 7) : 4 * (xcd & 1) + (bi & 3);                                 \
  const int kar = spare ? KARR : KARR + 3 * KCH;                                                                             \
  const int s0 = SKEW == 0 ? 0 : SKEW == 1 ? (cb * 3 + rp * 5 + (rp >> 1)) % 10 : SKEW == 2 ? 5 * ((cb + rp) & 1) : (xcd * 5 + (xcd >> 1)) % 10;             \
  const unsigned w2s = WS_W2T + (ct * 270 + w) * 1024;                                                                      \
  const int fhk = FL_H + (rt >> 1) * 14 + (lane < 14 ? lane : 0);                                                          \
  const unsigned hb0 = WS_HT + ((rt * 4 + (ct & 3)) * 54 + w) * 1024;                                                      \
  (void)c0; (void)ch3; (void)nch; (void)ch00; (void)nbat; (void)kar; (void)s0; (void)w2s; (void)fhk; (void)hb0
  const __amdgpu_buffer_rsrc_t rws = __builtin_amdgcn_make_buffer_rsrc(ws, 0, WS_END, 0x00020000);
  const int h = lane >> 5, g = lane >> 4, q = (lane & 15) >> 2, p = lane & 3, blk = g & 1;
  auto fw_flag = [&](int ct) { const int k = (lane >> 1) * 8 + w; return FL_W + (k < 54 ? k : w) * 10 + ((ct * 80 + (lane & 1) * 79) >> 6); };
  const unsigned k6 = w < 6 ? 49152u : 0u;
  v4u wf2[35];
  const unsigned w2v = lane * 16, w2v6 = w2v + (w < 6 ? 0u : OOB);
  auto load_w2_ = [&](unsigned w2s, int i0, int i1) {
#pragma unroll
    for (int i = 0; i < 35; ++i)
      if (i >= i0 && i < i1) wf2[i] = ld16(rws, i >= 30 ? w2v6 : w2v, w2s + (i % 5) * (54 * 1024) + (i / 5) * 8192);
  };
  auto warm_w2 = [&](int c2, u64 tag) {
    wait_flags(ws, fw_flag(c2), tag);
    load_w2_(WS_W2T + (c2 * 270 + w) * 1024, 0, 35);
#pragma unroll
    for (int i = 0; i < 35; ++i) GOT(wf2[i]);
  };
  v4u hf[7], junk = {};
  auto load_h = [&](unsigned base) {
#pragma unroll
    for (int i = 0; i < 7; ++i) hf[i] = ld16(rws, w2v, base + (i < 6 ? i * 8192u : k6));
  };
  unsigned tps[3], tos[3], tys[3], tbs[3], hb1, hb2, hb3, tsel[3], ovb, prb, ylb, fy_off;
  LAS char* Pw;
  auto setup_c = [&](int rt, int ct) {
#pragma unroll
    for (int i = 0; i < 3; ++i) {
      const int id0 = i == 0 ? w : i == 1 ? w + 8 : w + 15, id = id0 < 20 ? id0 : 0, pt = id / 5, n = id % 5, t0 = ((pt + ct) & 3) * 16;
      tps[i] = (pt == 3 ? 0 : pt) * GBUF + n * GT;
      tos[i] = ((rt * 64 + t0) * D + ct * 80 + 16 * n) * 2;
      tys[i] = WS_YT + (ct * 80 + 16 * n) * 4096 + rt * 128 + (t0 >> 5) * 64;
      tsel[i] = t0 & 16;
      tbs[i] = (ct * 80 + 16 * n) * 2;
    }
    hb1 = WS_HT + ((rt * 4 + ((ct + 1) & 3)) * 54 + w) * 1024; hb2 = WS_HT + ((rt * 4 + ((ct + 2) & 3)) * 54 + w) * 1024; hb3 = WS_HT + ((rt * 4 + ((ct + 3) & 3)) * 54 + w) * 1024;
    asm volatile("" ::"s"(tps[0]), "s"(tps[1]), "s"(tps[2]), "s"(tos[0]), "s"(tos[1]), "s"(tos[2]), "s"(tys[0]), "s"(tys[1]), "s"(tys[2]), "s"(tbs[0]), "s"(tbs[1]), "s"(tbs[2]),
                 "s"(hb1), "s"(hb2), "s"(hb3), "s"(tsel[0]), "s"(tsel[1]), "s"(tsel[2]));
    ovb = ((lane >> 2) * D + 4 * p) * 2;
    prb = p * 256 + (((lane >> 2) + 4 * p) & 15) * 16;
    ylb = (lane & 15) * 4096 + g * 16;
    fy_off = WS_FLAG + 128u * (FL_Y + ct * 28 + (lane < 28 ? lane : 0));
    unsigned pw_ = w * GW + g * 256 + (((lane & 15) + 4 * g) & 15) * 16;
    GOT(ovb); GOT(prb); GOT(ylb); GOT(fy_off); GOT(pw_);
    Pw = S + pw_;
    PIN();
  };
  u64 tag, fh = 0;
  if (w >= 4) {
    IDS();
    int sn = s0;
    const int u0 = spare ? 448 + 3 * sp : 2 * gi;
    const v4u sx = mk_srd(x, L * D * 2), sw1 = mk_srd(w1, D * F * 2);
    unsigned vx[4], vw[4];
    auto ring_setup = [&]() {
      int ln = lane;
      asm volatile("" : "+v"(ln));
#pragma unroll
      for (int j = 0; j < 4; ++j) {
        const int rx = 8 * j + (ln >> 3);
        vx[j] = (rp * 128 + rx) * (D * 2) + 16 * ((ln & 7) ^ ((rx >> 1) & 7)) - 1024 * j;
        vw[j] = (4 * j + (ln >> 4)) * (F * 2) + c0 * 2 + 16 * ((ln & 15) ^ (((ln >> 4) << 2) | j)) - 1024 * j;
      }
    };
    auto quarter = [&](int st, int slot, int k) {
      const unsigned so = st * 128 + k * (32 * D * 2), sv = st * (64 * F * 2) + k * (16 * F * 2);
      SET_M0(slot * SLOT + k * 4096);
      DMA(sx, vx[0], so, 0); DMA(sx, vx[1], so, 1024); DMA(sx, vx[2], so, 2048); DMA(sx, vx[3], so, 3072);
      SET_M0(slot * SLOT + 16384 + k * 4096);
      DMA(sw1, vw[0], sv, 0); DMA(sw1, vw[1], sv, 1024); DMA(sw1, vw[2], sv, 2048); DMA(sw1, vw[3], sv, 3072);
    };
    if (w == 7) {
      const __amdgpu_buffer_rsrc_t rx = __builtin_amdgcn_make_buffer_rsrc((void*)x, 0, L * D * 2, 0x00020000),
                                   rw2 = __builtin_amdgcn_make_buffer_rsrc((void*)w2, 0, D * F * 2, 0x00020000);
      const u64 ep = ld_word(ws, 0);
      v4u xv[10], wv[3][4];
#pragma unroll
      for (int i = 0; i < 10; ++i) xv[i] = __builtin_amdgcn_raw_buffer_load_b128(rx, (lane >> 3) * (D * 2) + (lane & 7) * 16 + i * 128, b * (8 * D * 2), 0);
#pragma unroll
      for (int k = 0; k < 3; ++k) {
        const int u = u0 + k;
        const unsigned vo = (lane >> 3) * (D * 2) + (lane & 7) * 16 + ((k < 2 || spare) && u < 540 ? 0u : OOB);
#pragma unroll
        for (int i = 0; i < 4; ++i) wv[k][i] = __builtin_amdgcn_raw_buffer_load_b128(rw2, vo, ((u / 10) * 32 + 8 * i) * (D * 2) + (u % 10) * 128, 0);
      }
      PIN();
      ring_setup();
      const u64 tg = next_tag(ep);
      lds_put(TAGL, (unsigned)tg); lds_put(TAGL + 4, (unsigned)(tg >> 32));
      lds_set(S, TAGL + 8, 1);
      GOT(pf0); GOT(pf1);
      float sm[2] = {}, sq[2] = {};
#pragma unroll
      for (int i = 0; i < 10; ++i) { sm[i & 1] = sum8(xv[i], sm[i & 1]); sq[i & 1] = dot8(xv[i], xv[i], sq[i & 1]); }
      float sum = sm[0] + sm[1], ssq = sq[0] + sq[1];
#pragma unroll
      for (int m = 1; m < 8; m <<= 1) { sum += __shfl_xor(sum, m); ssq += __shfl_xor(ssq, m); }
      const float mean = sum * (1.f / D), var = fmaxf(ssq * (1.f / D) - mean * mean, 0.f);
      const unsigned ms = pk(mean, __builtin_amdgcn_sqrtf(rbf(rbf(var) + 1e-5f)));
      if ((lane & 7) == 0) __hip_atomic_store((GAS u64*)(ws + WS_ST + (lane >> 3) * 2048 + b * 8), (tg << 32) | ms, __ATOMIC_RELAXED, __HIP_MEMORY_SCOPE_AGENT);
      STAMP(1);
      LAS char* U = S + (spare ? W2U_S : W2U_G);
      LAS char* B = U + (4 * (blk + 4 * (g >> 1)) + q) * 128 + 8 * p;
      GOT(wv[2][0]); GOT(wv[2][1]); GOT(wv[2][2]); GOT(wv[2][3]);
#pragma unroll
      for (int k = 0; k < 3; ++k) {
        const int u = u0 + k;
        if ((k < 2 || spare) && u < 540) {
#pragma unroll
          for (int i = 0; i < 4; ++i) *(LAS v4u*)(U + i * 1024 + lane * 16) = wv[k][i];
#pragma unroll
          for (int n = 0; n < 4; ++n) st16(rws, trd2(B + 32 * n, B + 32 * n + 1024), lane * 16 + WS_W2T + (((u % 10) * 4 + n) * 54 + u / 10) * 1024);
        }
      }
    } else if (!spare) {
      ring_setup();
#pragma unroll 1
      for (int t = wq; t < 12; t += 3) {
        if (t >= 9) { WAITV(8); lds_put(CNT + 28, 1); lds_put(CNT + 16 + 4 * wq, 1); }
        quarter(sn + (t >> 2) >= 10 ? sn + (t >> 2) - 10 : sn + (t >> 2), t >> 2, t & 3);
      }
      WAITV(8);
      STAMP(1);
    } else if (sp < 27) {
      const __amdgpu_buffer_rsrc_t rw1 = __builtin_amdgcn_make_buffer_rsrc((void*)w1, 0, D * F * 2, 0x00020000);
      v4u v[27];
#pragma unroll
      for (int i = 0; i < 27; ++i) v[i] = __builtin_amdgcn_raw_buffer_load_b128(rw1, (216 * wq + 8 * i + (lane >> 3)) * (F * 2) + (lane & 7) * 16, sp * 128, 0);
      PIN();
      f2 a[4] = {};
#pragma unroll
      for (int i = 0; i < 27; ++i)
#pragma unroll
        for (int e = 0; e < 4; ++e) a[e] += unpk(v[i][e]);
      *(LAS f4*)(S + (wq * 64 + lane) * 32) = f4{a[0].x, a[0].y, a[1].x, a[1].y};
      *(LAS f4*)(S + (wq * 64 + lane) * 32 + 16) = f4{a[2].x, a[2].y, a[3].x, a[3].y};
      lds_put(CNT + 16 + 12, 1);
      if (lds_arrive(S, 1, wq)) {
        float t = 0.f;
#pragma unroll
        for (int i = 0; i < 24; ++i) t += *(LAS float*)(S + i * 256 + lane * 4);
        __hip_atomic_store((GAS u64*)(ws + WS_CS + (sp * 64 + lane) * 8), (get_tag(S) << 32) | __float_as_uint(t), __ATOMIC_RELAXED, __HIP_MEMORY_SCOPE_AGENT);
      }
    }
    int fwk = fw_flag(ct);
    GOT(fwk);
    STAMP(2);
    if (spare) {
      tag = get_tag(S);
      if (w == 7) {
        WAITV(0);
        if (lane < 3 && u0 + lane < 540) put_flag(ws, FL_W + u0 + lane, tag);
      }
      if (WARMB) warm_w2(2 * (bi >> 3), tag);
      if (WARMB > 1 && (bi >> 3) != 3) warm_w2(2 * (bi >> 3) + 1, tag);
      wait_flags(ws, fwk, tag);
      load_w2_(w2s, 0, 35);
    } else {
      const int t4 = tid - 256;
      const int fxk = FL_X + (lane & 15) * 10 + ((g == 2 && nch == 3 ? ch3 : ch00 + (g == 1)) >> 6);
      bar();
      STAMP(6);
      tag = rd_tag(S);
      sn = sn >= 7 ? sn - 7 : sn + 3;
      auto next = [&](int slot) { quarter(sn, slot, wq); sn = sn == 9 ? 0 : sn + 1; };
      next(3);
      WAITV(8);
      if (w == 7 && lane < 2) put_flag(ws, FL_W + u0 + lane, tag);
      bar_raw();
#pragma unroll 1
      for (int n = 0; n < 4; ++n) {
        next(n);
        WAITV(8);
        bar_raw();
      }
      next(0);
      u64 px = ld_hidden(ws, WS_FLAG + 128u * fxk);
      WAITV(9);
      bar_raw();
      STAMP(3);
      next(1);
      WAITV(8);
      bar_raw();
      STAMP(4);
      GOT(px);
      check_flags(ws, fxk, px, tag);
      v2u gr[8];
      v4u un[3];
#pragma unroll
      for (int i = 0; i < 8; ++i) gr[i] = __builtin_amdgcn_raw_buffer_load_b64(rws, t4 * 8, WS_ST + i * 2048, 16);
#pragma unroll
      for (int j = 0; j < 3; ++j) un[j] = ld16(rws, t4 * 16 + (j < nch ? 0u : OOB), WS_XT + (j < 2 ? ch00 + j : ch3) * 4096);
      u64 pw = ld_hidden(ws, WS_FLAG + 128u * fwk);
      WAITV(12);
      bar_raw();
      STAMP(5);
      __builtin_amdgcn_s_setprio(BPRIO);
      {
        unsigned bad = 0;
#pragma unroll
        for (int i = 0; i < 8; ++i) bad |= gr[i].y ^ (unsigned)tag;
        if (!__all(bad == 0)) {
#pragma unroll
          for (int i = 0; i < 8; ++i) gr[i].x = (unsigned)wait_granule(ws, WS_ST + i * 2048 + t4 * 8, tag);
        }
        float rr[8];
#pragma unroll
        for (int e = 0; e < 8; ++e) rr[e] = __builtin_amdgcn_rcpf(bfhi(gr[e].x));
        asm volatile("s_waitcnt vmcnt(0)" : "+v"(rr[0]), "+v"(rr[1]), "+v"(rr[2]), "+v"(rr[3]), "+v"(rr[4]), "+v"(rr[5]), "+v"(rr[6]), "+v"(rr[7]), "+v"(pw), "+v"(un[0]), "+v"(un[1]), "+v"(un[2])::"memory");
        check_flags(ws, fwk, pw, tag);
        load_w2_(w2s, 0, NB0);
        PIN();
        STAMP(1);
#pragma unroll
        for (int e = 0; e < 4; ++e) {
          const float n0 = smul(bflo(gr[2 * e].x), rr[2 * e]), n1 = smul(bflo(gr[2 * e + 1].x), rr[2 * e + 1]);
#pragma unroll
          for (int j = 0; j < 3; ++j) un[j][e] = pk(sfms(bflo(un[j][e]), rr[2 * e], n0), sfms(bfhi(un[j][e]), rr[2 * e + 1], n1));
        }
        LAS char* U = S + UCP + 32 + t4 * 16;
        if (t4 < 56) *(LAS v4u*)(S + (t4 < 24 ? UCP + (t4 >> 1) * UST + (t4 & 1) * 16 : KARR + 5 * KCH + (t4 - 24) * 16)) = v4u{0u, 0u, 0u, 0u};
#pragma unroll
        for (int j = 0; j < 3; ++j) *(LAS v4u*)(U + j * UCH) = un[j];
        lds_put(CNT + 16 * 7 + 4 * wq, 1);
        lds_wait(S, 7);
#pragma unroll
        for (int j = 0; j < 3; ++j) {
          const v4u d = un[j]; const v2u pv = *(LAS v2u*)(U + j * UCH - 8);
          const unsigned a0 = __builtin_amdgcn_alignbit(pv.y, pv.x, 16), a1 = __builtin_amdgcn_alignbit(d.x, pv.y, 16), a2 = __builtin_amdgcn_alignbit(d.y, d.x, 16),
                         a3 = __builtin_amdgcn_alignbit(d.z, d.y, 16), a4 = __builtin_amdgcn_alignbit(d.w, d.z, 16);
          *(LAS v4u*)(U + j * UCH + UST) = v4u{a1, a2, a3, a4};
          *(LAS v4u*)(U + j * UCH + 2 * UST) = v4u{pv.y, d.x, d.y, d.z};
          *(LAS v4u*)(U + j * UCH + 3 * UST) = v4u{a0, a1, a2, a3};
        }
      }
      if (nch == 3) wait_flags(ws, FL_K + 4 * gq + gm / 6, tag);
      const v4u k3 = ld16(rws, t4 * 16, WS_K3 + ch3 * 4096);
      ORDER();
      load_w2_(w2s, NB0, NB1);
      *(LAS v4u*)(S + KARR + 5 * KCH + 512 + t4 * 16) = k3;
      lds_put(CNT + 16 * 8 + 4 * wq, 1);
      STAMP(12);
      LAS char* Kp = S + kar + 512 + 32 * ((lane & 15) - (g >> 1)) + 16 * blk - 64 * wq - 256;
      LAS char* Up = S + UCP + q * UST + 32 + 32 * (g >> 1) - 16 * blk + 8 * p + 64 * wq - 8;
      LAS char* const Pp0 = S + wq * 8192 + g * 256 + (((lane & 15) ^ (2 * g)) * 16);
      lds_wait(S, 8);
      v4u kf[8], kg[8], ua[2], ub[2];
      auto rd_u = [&](int o, int s) {
        ua[s & 1] = trd2(Up + o * UCH + 512 * s + 8, Up + o * UCH + 512 * s); ub[s & 1] = trd2(Up + o * UCH + 512 * (7 - s) + 264, Up + o * UCH + 512 * (7 - s) + 256);
        kg[s] = *(LAS v4u*)(Kp + o * KCH + 512 * s);
      };
      rd_u(0, 0);
#pragma unroll
      for (int e = 7; e >= 0; --e) kf[e] = *(LAS v4u*)(Kp + 512 * e + 256);
      __builtin_amdgcn_s_setprio(1);
      lds_wait(S, 6);
      STAMP(13);
      auto channel = [&](int cj, bool last) {
        f4 acc[8];
        PIN();
#pragma unroll
        for (int s = 0; s < 8; ++s) {
          if (s < 7) rd_u(0, s + 1);
          if (last) load_w2_(w2s, NB1 + 2 * s, NB1 + 2 * s + 2);
          else if (s > 0) kf[8 - s] = *(LAS v4u*)(Kp + KCH + 512 * (8 - s) + 256);
#pragma unroll
          for (int R = 7; R >= s; --R) acc[R] = __builtin_amdgcn_mfma_f32_16x16x32_bf16(asbf(ua[s & 1]), asbf(kf[R - s]), s ? acc[R] : f4{0.f, 0.f, 0.f, 0.f}, 0, 0, 0);
#pragma unroll
          for (int R = 7 - s; R < 8; ++R) acc[R] = __builtin_amdgcn_mfma_f32_16x16x32_bf16(asbf(ub[s & 1]), asbf(kg[R - 7 + s]), acc[R], 0, 0, 0);
#pragma unroll
          for (int i = 0; i < 7; ++i) { __builtin_amdgcn_sched_group_barrier(0x8, 1, 0); __builtin_amdgcn_sched_group_barrier(0x90, 1, 0); }
          PIN();
        }
        if (!last) {
          kf[0] = *(LAS v4u*)(Kp + KCH + 256);
          rd_u(1, 0);
        } else if (cj == 2) lds_wait_ge(S, CNT + 16 * 9 + 4 * p, 1);
        PIN();
#pragma unroll
        for (int R = 0; R < 8; ++R) *(LAS f4*)(Pp0 + (cj & 1) * SLOT + R * 1024) = acc[R];
        lds_put(CNT + 16 * 2 + 4 * wq, cj + 1);
        Kp += KCH; Up += UCH;
      };
#pragma unroll 1
      for (int cj = 0; cj < nch - 1; ++cj) channel(cj, false);
      fh = ld_word(ws, WS_FLAG + 128u * fhk);
      channel(nch - 1, true);
    }
    STAMP(7);
    __builtin_amdgcn_s_setprio(CPRIO);
    check_flags(ws, fhk, fh, tag);
    load_h(hb0);
  } else {
    IDS();
    const int wf = w >> 1, tg = w & 1;
    unsigned xa[4], wa[2][2], pn[6];
    unsigned lgn, dskn;
    auto fetch = [&](int bt) {
      const int ch = ch00 + 2 * bt + wf, o = ch * N + tg * 64 + lane;
      pn[0] = Are[o]; pn[1] = Aim[o]; pn[2] = Bre[o]; pn[3] = Bim[o]; pn[4] = Cre[o]; pn[5] = Cim[o];
      lgn = logdt[ch]; dskn = dskip[ch];
      PIN();
    };
    fetch(0);
    int fwk = fw_flag(ct);
    GOT(fwk);
#pragma unroll
    for (int i = 0; i < 4; ++i) {
      const int row = 64 * tg + (lane & 31), c = 2 * wf + (i >> 1), t2 = i & 1;
      xa[i] = (row * 128 + 16 * (h ^ ((row >> 1) & 7))) ^ (32 * i);
      wa[i >> 1][i & 1] = 16384 + 256 * (8 * h + 4 * t2 + q) + 64 * (c ^ q) + 16 * ((2 * blk + (p >> 1)) ^ (2 * h + t2)) + 8 * (p & 1);
      GOT(xa[i]); GOT(wa[i >> 1][i & 1]);
    }
    PIN();
#pragma unroll 1
    for (int bt = 0; bt < nbat; ++bt) {
      const unsigned p0 = pn[0], p1 = pn[1], p2 = pn[2], p3 = pn[3], p4 = pn[4], p5 = pn[5];
      const float lg = bflo(lgn), dsk = bflo(dskn);
      if (bt + 1 < nbat) fetch(bt + 1);
      if (bt) lds_wait_ge(S, CNT + 16 * 5 + 4 * (w ^ 1), bt);
      LAS unsigned* T = (LAS unsigned*)(S + TAB + w * TW) + lane;
      LAS char* Tr = S + TAB + w * TW + g * 16 + (lane & 15) * TROW;
      constexpr int R = TROW / 4;
      const float dt = rbf(__expf(lg)), a = dt * bflo(p0) * 1.4426950408889634f;
      float th = dt * bflo(p1) * 0.15915494309189535f;
      th -= __builtin_rintf(th);
      const f2 e1 = cexpl(a, th, 1.f), e128 = cexpl(a, th, 128.f), e8 = cexpl(a, -th, 8.f);
      const cm m1 = mkm(e1), m128 = mkm(e128), m8 = mkm(e8), m16 = mkm(cmul(e8, m8));
      const f2 e2 = cmul(e1, m1);
      const cm m2 = mkm(e2), m4 = mkm(cmul(e2, m2));
      f2 y = cmul(f2{bflo(p4), -bflo(p5)}, mkm(f2{dt, dt} * f2{bflo(p2), -bflo(p3)})), y2 = cmul(y, m8), t = cexpl(a, th, 1024.f);
#pragma unroll
      for (int i = 0; i < 8; ++i) {
        T[(2 * i) * R] = pk2(y); T[(2 * i + 1) * R] = pk2(y2); T[(16 + i) * R] = pk2(t);
        if (i < 7) { y = cmul(y, m16); y2 = cmul(y2, m16); }
        t = cmul(t, m128);
      }
      T[24 * R] = pk2(t);
      v4u fy[4], fa[4][2];
      f4 ka[3] = {};
#pragma unroll
      for (int ks = 0; ks < 4; ++ks) { fy[ks] = *(LAS v4u*)(Tr + ks * 64); fa[ks][0] = *(LAS v4u*)(Tr + 16 * TROW + ks * 64); }
      PIN();
      f2 u = {1.f, 0.f}, v = e128, z = cmul(e128, m128);
#pragma unroll
      for (int i = 0; i < 8; ++i) {
        T[i * R] = pk2(u); T[(8 + i) * R] = pk2(v);
        if (i < 7) { u = cmul(u, m1); v = cmul(v, m1); }
        if (i < 2) {
          const f2 z2 = cmul(z, m2);
          T[(16 + 4 * i) * R] = pk2(z); T[(17 + 4 * i) * R] = pk2(z2); T[(18 + 4 * i) * R] = pk2(cmul(z, m4)); T[(19 + 4 * i) * R] = pk2(cmul(z2, m4));
        } else if (i < 6) {
          T[(20 + 2 * i) * R] = pk2(z); T[(21 + 2 * i) * R] = pk2(cmul(z, m4));
        }
        if (i < 5) z = cmul(z, m128);
      }
      PIN();
#pragma unroll
      for (int ks = 0; ks < 4; ++ks) ka[2] = __builtin_amdgcn_mfma_f32_16x16x32_bf16(asbf(fa[ks][0]), asbf(fy[ks]), ka[2], 0, 0, 0);
#pragma unroll
      for (int ks = 0; ks < 4; ++ks) { fa[ks][0] = *(LAS v4u*)(Tr + ks * 64); fa[ks][1] = *(LAS v4u*)(Tr + 16 * TROW + ks * 64); }
      PIN();
#pragma unroll
      for (int ks = 0; ks < 4; ++ks)
#pragma unroll
        for (int i = 0; i < 2; ++i) ka[i] = __builtin_amdgcn_mfma_f32_16x16x32_bf16(asbf(fa[ks][i]), asbf(fy[ks]), ka[i], 0, 0, 0);
      if (bt == 0) STAMP(1);
      {
        LAS char* X = S + TAB + w * TW + 4608 + lane * 16;
        *(LAS f4*)X = tg ? ka[0] : ka[1];
        *(LAS f4*)(X + 1024) = ka[2];
        lds_put(CNT + 16 * 4 + 4 * w, bt + 1);
        lds_wait_ge(S, CNT + 16 * 4 + 4 * (w ^ 1), bt + 1);
        LAS char* Y = S + TAB + (w ^ 1) * TW + 4608 + lane * 16;
        f4 km = (tg ? ka[1] : ka[0]) + *(LAS f4*)Y, k2 = ka[2] + *(LAS f4*)(Y + 1024);
        GOT(km); GOT(k2);
        lds_put(CNT + 16 * 5 + 4 * w, bt + 1);
        const int c = lane & 15;
        LAS char* K = S + kar + (2 * bt + wf) * KCH;
        LAS u16* T1 = (LAS u16*)(S + TAB + w * TW);
        const unsigned q01 = pk2(k2.xy), q23 = pk2(k2.zw);
        if (!tg) {
          if (lane == 0) km.x += dsk;
          *(LAS v2u*)(K + 512 + 256 * (g >> 1) + 16 * c + 8 * blk) = v2u{pk2(km.xy), pk2(km.zw)};
          LAS u16* T3 = T1 + 64 * g + c;
          T3[0] = (u16)q01; T3[16] = (u16)(q01 >> 16); T3[32] = (u16)q23; T3[48] = (u16)(q23 >> 16);
          ORDER();
          const unsigned c0_ = T1[lane], c1 = T1[1 + lane], d0 = T1[64 + lane], d1 = T1[65 + lane];
          if (h) *(LAS v4u*)(K + 16 * (lane & 31)) = v4u{0u, 0u, 0u, 0u};
          *(LAS v4u*)(K + 2560 + 16 * lane) = v4u{c0_ * 0x10001u, c0_ * 0x10001u, ((lane & 1) ? c1 : c0_) | (c1 << 16), c1 * 0x10001u};
          *(LAS v4u*)(K + 3584 + 16 * lane) = v4u{d0 * 0x10001u, d0 * 0x10001u, ((lane & 1) ? d1 : d0) | (d1 << 16), d1 * 0x10001u};
        } else {
          const unsigned p01 = pk2(km.xy), p23 = pk2(km.zw);
          if (g < 2) *(LAS v2u*)(T1 + 64 * g + 4 * c) = v2u{p01, p23};
          else { *(LAS unsigned*)(T1 + 64 * g + 2 * c) = p01; *(LAS unsigned*)(T1 + 64 * g + 32 + 2 * c) = p23; }
          if (lane == 0) T1[256] = (u16)q01;
          ORDER();
          const v2u a1 = *(LAS v2u*)(T1 + 4 * (lane & 31));
          const unsigned a4 = T1[4 * (lane & 31) + 4], b0 = *(LAS unsigned*)(T1 + 128 + 2 * lane), b2_ = T1[130 + 2 * lane];
          if (!h) *(LAS v4u*)(K + 1024 + 16 * lane) = v4u{(a1.x & 0xffffu) * 0x10001u, (a1.x >> 16) | (a1.y << 16), (a1.y & 0xffffu) * 0x10001u, (a1.y >> 16) | (a4 << 16)};
          *(LAS v4u*)(K + 1536 + 16 * lane) = v4u{(b0 & 0xffffu) * 0x10001u, b0, (b0 >> 16) * 0x10001u, b2_ * 0x10001u};
        }
      }
    }
    if (cb < 10 && !spare) {
      lds_wait(S, 1);
      v2u xt[8];
#pragma unroll
      for (int m = 0; m < 8; ++m) {
        const int rr = 32 * g + 4 * m + q;
        xt[m] = trd(S + 128 * rr + 16 * ((2 * w + (p >> 1)) ^ ((rr >> 1) & 7)) + 8 * (p & 1));
      }
      LAS char* X = S + TAB + w * TW;
#pragma unroll
      for (int k = 0; k < 4; ++k) *(LAS v4u*)(X + (lane & 15) * 272 + g * 64 + k * 16) = v4u{xt[2 * k].x, xt[2 * k].y, xt[2 * k + 1].x, xt[2 * k + 1].y};
#pragma unroll
      for (int k = 0; k < 4; ++k)
        st16(rws, *(LAS v4u*)(X + (4 * k + g) * 272 + (lane & 15) * 16), WS_XT + (s0 * 64 + 16 * w + 4 * k + g) * 4096 + rp * 256 + (lane & 15) * 16);
    }
    STAMP(2);
    if (!spare) {
      f16v acc[2][2] = {};
      v4u fx[4][2], fw[4][2];
      auto rd = [&](int set, int so, int ks) {
        fw[set][0] = trd2(S + wa[0][0] + so + 4096 * ks, S + wa[0][1] + so + 4096 * ks);
        fx[set][0] = *(LAS v4u*)(S + xa[ks] + so);
        fx[set][1] = *(LAS v4u*)(S + xa[ks] + so + 4096);
        fw[set][1] = trd2(S + wa[1][0] + so + 4096 * ks, S + wa[1][1] + so + 4096 * ks);
      };
      auto mm = [&](int set) {
#pragma unroll
        for (int i = 0; i < 4; ++i) acc[i >> 1][i & 1] = __builtin_amdgcn_mfma_f32_32x32x16_bf16(asbf(fw[set][i >> 1]), asbf(fx[set][i & 1]), acc[i >> 1][i & 1], 0, 0, 0);
#pragma unroll
        for (int i = 0; i < 3; ++i) { __builtin_amdgcn_sched_group_barrier(0x8, 1, 0); __builtin_amdgcn_sched_group_barrier(0x100, 2, 0); }
        __builtin_amdgcn_sched_group_barrier(0x8, 1, 0);
        PIN();
      };
      u64 fw_seen = 0, g0 = 0, g1 = 0, gc = 0;
      unsigned b1v = 0;
      const unsigned o0 = WS_ST + (lane & 7) * 2048 + (rp * 16 + 8 * tg + ((lane & 31) >> 3)) * 8, oc = WS_CS + (c0 + 64 * wf + lane) * 8;
      asm volatile("" : "+v"(acc[0][0]), "+v"(acc[0][1]), "+v"(acc[1][0]), "+v"(acc[1][1]));
      bar();
      STAMP(6);
      tag = rd_tag(S);
      rd(0, 0, 0); rd(1, 0, 1);
      PIN();
      auto flip = [&]() {
#pragma unroll
        for (int i = 0; i < 4; ++i) { xa[i] ^= 2 * SLOT; wa[i >> 1][i & 1] ^= 2 * SLOT; }
      };
#pragma unroll 1
      for (int n = 0; n < 4; ++n) {
        if (n == 3) { fw_seen = ld_hidden(ws, WS_FLAG + 128u * fwk); g0 = ld_hidden(ws, o0); g1 = ld_hidden(ws, o0 + 32); gc = ld_hidden(ws, oc);
          asm volatile("global_load_ushort %0, %1, %2" : "=v"(b1v) : "v"((c0 + 64 * wf + lane) * 2), "s"(b1) : "memory"); }
        rd(2, 0, 2); mm(0);
        rd(3, 0, 3); mm(1);
        rd(0, SLOT, 0); mm(2);
        rd(1, SLOT, 1); mm(3);
        if (n == FXN) WAITV(0);
        bar_raw();
        if (n == FXN && w == 1 && cb < 10 && lane == 0) put_flag(ws, FL_X + rp * 10 + (SKEW == 1 ? s0 : cb), tag);
        rd(2, SLOT, 2); mm(0);
        rd(3, SLOT, 3); mm(1);
        flip();
        rd(0, 0, 0); mm(2);
        rd(1, 0, 1); mm(3);
        bar_raw();
      }
      WAITV(0); GOT(fw_seen); GOT(g0); GOT(g1); GOT(gc); GOT(b1v);
      if (!tag_ok(gc, tag)) gc = wait_granule(ws, oc, tag);
      *(LAS unsigned*)(S + COLT + w * 512 + 256 + lane * 4) = b1v << 16;
      *(LAS unsigned*)(S + COLT + w * 512 + lane * 4) = (unsigned)gc;
      if (!tag_ok(g0, tag)) g0 = wait_granule(ws, o0, tag);
      if (!tag_ok(g1, tag)) g1 = wait_granule(ws, o0 + 32, tag);
      check_flags(ws, fwk, fw_seen, tag);
      PIN();
      rd(2, 0, 2); mm(0);
      rd(3, 0, 3); mm(1);
      rd(0, SLOT, 0); mm(2);
      rd(1, SLOT, 1); mm(3);
      rd(2, SLOT, 2); mm(0);
      rd(3, SLOT, 3); mm(1);
      mm(2);
      mm(3);
      STAMP(3);
      const unsigned hv = WS_HT + (((rp * 8 + 4 * tg + blk) * 54 + (c0 >> 5) + 2 * wf) * 64 + (lane & 15) + 16 * h) * 16 + ((cb == 13 && wf == 0) ? OOB : 0u);
#pragma unroll
      for (int c = 0; c < 2; ++c) {
        f4 cs[4], bb[4];
#pragma unroll
        for (int a = 0; a < 4; ++a) { LAS char* ct_ = S + COLT + w * 512 + (32 * c + 8 * a + 4 * h) * 4; cs[a] = *(LAS f4*)ct_; bb[a] = *(LAS f4*)(ct_ + 256); }
#pragma unroll
        for (int tt = 0; tt < 2; ++tt) {
          const float rs = __builtin_amdgcn_rcpf(bfhi((unsigned)(tt ? g1 : g0))), mr = -bflo((unsigned)(tt ? g1 : g0)) * rs;
          unsigned o[8];
#pragma unroll
          for (int a = 0; a < 4; ++a) {
#pragma unroll
            for (int e = 0; e < 2; ++e) {
              const f2 v = fma2(f2{acc[c][tt][4 * a + 2 * e], acc[c][tt][4 * a + 2 * e + 1]}, f2{rs, rs}, fma2(f2{mr, mr}, e ? cs[a].zw : cs[a].xy, e ? bb[a].zw : bb[a].xy));
              const f2 z = v * fma2(v * v, f2{-0.10294324f, -0.10294324f}, f2{-2.30220819f, -2.30220819f});
              const f2 dn = f2{__builtin_amdgcn_exp2f(z.x), __builtin_amdgcn_exp2f(z.y)} + f2{1.f, 1.f};
              o[2 * a + e] = pk2(v * f2{__builtin_amdgcn_rcpf(dn.x), __builtin_amdgcn_rcpf(dn.y)});
            }
          }
          load_w2_(w2s, NA * (2 * c + tt), NA * (2 * c + tt + 1));
          st16(rws, v4u{o[0], o[1], o[2], o[3]}, hv + (tt * 108 + c) * 1024);
          st16(rws, v4u{o[4], o[5], o[6], o[7]}, hv + (tt * 108 + c) * 1024 + 512);
          PIN();
        }
      }
      STAMP(4);
      lds_put(CNT + 16 * 6 + 4 * wq, 1);
      load_w2_(w2s, 4 * NA, 4 * NA + 8);
      WAITV(8);
      if (lds_arrive(S, 0, wq) && lane == 0) put_flag(ws, FL_H + gi, tag);
      STAMP(5);
      load_w2_(w2s, 4 * NA + 8, 35);
      auto publish = [&](int j) {
        lds_wait_ge(S, CNT + 16 * 2 + 4 * p, j + 1);
        const unsigned pa = (j & 1) * SLOT + (tid >> 5) * 1024 + (((tid >> 1) & 15) ^ (4 * (tid & 1))) * 16 + (tid & 1) * 512;
        f4 lo[4], hi[4], l, m;
#pragma unroll
        for (int v = 0; v < 4; ++v) { lo[v] = *(LAS f4*)(S + pa + v * 8192); hi[v] = *(LAS f4*)(S + (pa ^ 32u) + 256 + v * 8192); }
#pragma unroll
        for (int e = 0; e < 4; ++e) { l[e] = sadd(sadd(lo[0][e], lo[1][e]), sadd(lo[2][e], lo[3][e])); m[e] = sadd(sadd(hi[0][e], hi[1][e]), sadd(hi[2][e], hi[3][e])); }
        GOT(l); GOT(m);
        lds_put(CNT + 16 * 9 + 4 * wq, j + 1);
        st16(rws, v4u{pk2(l.xy), pk2(l.zw), pk2(m.xy), pk2(m.zw)}, tid * 16 + WS_YT + (j < 2 ? ch00 + j : ch3) * 4096);
      };
      auto first_h = [&]() {
        const v4u sw = mk_srd(ws, WS_END);
#pragma unroll
        for (int i = 0; i < 7; ++i) asm volatile("buffer_load_dwordx4 %0, %1, %2, %3 offen sc1" : "+v"(junk) : "v"(w2v), "s"(sw), "s"(hb0 + 4096 + (i < 6 || w < 2 ? i * 8192u : 0u)) : "memory");
        load_h(hb0);
        PIN();
      };
      publish(0);
      u64 f1 = ld_word(ws, WS_FLAG + 128u * fhk);
      if (nch == 3) publish(1);
      if (!__all(f1 == tag)) f1 = ld_word(ws, WS_FLAG + 128u * fhk);
      const bool early = __all(f1 == tag);
      __builtin_amdgcn_fence(__ATOMIC_ACQUIRE, "wavefront");
      if (early) first_h();
      publish(nch - 1);
      WAITV(0);
      if (lds_arrive(S, 10, wq) && lane == 0) put_flag(ws, FL_Y + gi, tag);
      if (!early) {
        wait_flags(ws, fhk, tag);
        load_h(hb0);
      }
    } else {
      lds_arrive(S, 6, wq);
      lds_wait(S, 6);
      tag = get_tag(S);
#pragma unroll
      for (int j = 0; j < 6; ++j) st16(rws, *(LAS v4u*)(S + KARR + j * KCH + 512 + tid * 16), tid * 16 + WS_K3 + (ch00 + j) * 4096);
      WAITV(0);
      if (lds_arrive(S, 0, wq) && lane == 0) put_flag(ws, FL_K + sp, tag);
      if (WARMA) warm_w2(2 * (bi >> 3), tag);
      wait_flags(ws, fwk, tag);
      load_w2_(w2s, 0, 35);
      wait_flags(ws, fhk, tag);
      load_h(hb0);
    }
    STAMP(7);
  }
  {
    STAMP(8);
    IDS();
    PIN();
    setup_c(rt, ct);
    const v4u sws = mk_srd(ws, WS_END);
    f4 acc[2][5];
#define GROUP(WAIT, A, j, OFF)                                                                                             \
    asm volatile("s_waitcnt vmcnt(" #WAIT ")\n\tv_mfma_f32_16x16x32_bf16 %0, %6, %5, %0\n\tv_mfma_f32_16x16x32_bf16 %1, %7, %5, %1\n\t"  \
                 "v_mfma_f32_16x16x32_bf16 %2, %8, %5, %2\n\tv_mfma_f32_16x16x32_bf16 %3, %9, %5, %3\n\t"                      \
                 "v_mfma_f32_16x16x32_bf16 %4, %10, %5, %4\n\tbuffer_load_dwordx4 %5, %11, %12, %13 offen sc1"                \
                 : "+v"(A[0]), "+v"(A[1]), "+v"(A[2]), "+v"(A[3]), "+v"(A[4]), "+v"(hf[j])                                  \
                 : "v"(wf2[5 * j]), "v"(wf2[5 * j + 1]), "v"(wf2[5 * j + 2]), "v"(wf2[5 * j + 3]), "v"(wf2[5 * j + 4]), "v"(w2v), "s"(sws), "s"(OFF) : "memory")
#define GROUP0(WAIT, A, OFF)                                                                                               \
    asm volatile("s_waitcnt vmcnt(" #WAIT ")\n\tv_mfma_f32_16x16x32_bf16 %0, %6, %5, 0\n\tv_mfma_f32_16x16x32_bf16 %1, %7, %5, 0\n\t"    \
                 "v_mfma_f32_16x16x32_bf16 %2, %8, %5, 0\n\tv_mfma_f32_16x16x32_bf16 %3, %9, %5, 0\n\t"                        \
                 "v_mfma_f32_16x16x32_bf16 %4, %10, %5, 0\n\tbuffer_load_dwordx4 %5, %11, %12, %13 offen sc1"                 \
                 : "=&v"(A[0]), "=&v"(A[1]), "=&v"(A[2]), "=&v"(A[3]), "=&v"(A[4]), "+v"(hf[0])                             \
                 : "v"(wf2[0]), "v"(wf2[1]), "v"(wf2[2]), "v"(wf2[3]), "v"(wf2[4]), "v"(w2v), "s"(sws), "s"(OFF) : "memory")
#define LAST(WAIT, A, j, C)                                                                                                \
    asm volatile("s_waitcnt vmcnt(" #WAIT ")\n\tv_mfma_f32_16x16x32_bf16 %0, %6, %5, " C(0) "\n\tv_mfma_f32_16x16x32_bf16 %1, %7, %5, " C(1) "\n\t" \
                 "v_mfma_f32_16x16x32_bf16 %2, %8, %5, " C(2) "\n\tv_mfma_f32_16x16x32_bf16 %3, %9, %5, " C(3) "\n\t"          \
                 "v_mfma_f32_16x16x32_bf16 %4, %10, %5, " C(4)                                                              \
                 : "+v"(A[0]), "+v"(A[1]), "+v"(A[2]), "+v"(A[3]), "+v"(A[4])                                               \
                 : "v"(hf[j]), "v"(wf2[5 * j]), "v"(wf2[5 * j + 1]), "v"(wf2[5 * j + 2]), "v"(wf2[5 * j + 3]), "v"(wf2[5 * j + 4]) : "memory")
#define C_ACC(i) "%" #i
#define C_ZERO(i) "0"
#define PUT(buf, A, n) *(LAS f4*)(Pw + (buf) * GBUF + (n) * GT) = A[n]
#pragma unroll
    for (int i = 0; i < 35; ++i) GOT(wf2[i]);
#pragma unroll
    for (int i = 0; i < 7; ++i) GOT(hf[i]);
    GOT(junk);
    STAMP(9);
    GROUP0(63, acc[0], hb1); GROUP(63, acc[0], 1, hb1 + 8192); GROUP(63, acc[0], 2, hb1 + 16384); GROUP(63, acc[0], 3, hb1 + 24576);
    GROUP(63, acc[0], 4, hb1 + 32768); GROUP(63, acc[0], 5, hb1 + 40960); GROUP(63, acc[0], 6, hb1 + k6);
    STAMP(11);
    if (!spare) lds_wait_ge(S, CNT + 16 * 9 + 4 * p, nch);
    else bar();
    GROUP0(6, acc[1], hb2); PUT(0, acc[0], 0); GROUP(6, acc[1], 1, hb2 + 8192); PUT(0, acc[0], 1); GROUP(6, acc[1], 2, hb2 + 16384); PUT(0, acc[0], 2);
    GROUP(6, acc[1], 3, hb2 + 24576); PUT(0, acc[0], 3); GROUP(6, acc[1], 4, hb2 + 32768); PUT(0, acc[0], 4); GROUP(6, acc[1], 5, hb2 + 40960); GROUP(6, acc[1], 6, hb2 + k6);
    STAMP(14);
    GROUP0(6, acc[0], hb3); PUT(1, acc[1], 0); GROUP(6, acc[0], 1, hb3 + 8192); PUT(1, acc[1], 1); GROUP(6, acc[0], 2, hb3 + 16384); PUT(1, acc[1], 2);
    GROUP(6, acc[0], 3, hb3 + 24576); PUT(1, acc[1], 3); GROUP(6, acc[0], 4, hb3 + 32768); PUT(1, acc[1], 4); GROUP(6, acc[0], 5, hb3 + 40960); GROUP(6, acc[0], 6, hb3 + k6);
    fh = ld_hidden(ws, fy_off);
    v2u xr[3], bb[3];
    {
      const v4u sx = mk_srd(x, L * D * 2), sb = mk_srd(b2, D * 2);
#pragma unroll
      for (int i = 0; i < 3; ++i)
        asm volatile("buffer_load_dwordx2 %0, %2, %3, %4 offen\n\tbuffer_load_dwordx2 %1, %5, %6, %7 offen" : "=&v"(xr[i]), "=&v"(bb[i]) : "v"(ovb), "s"(sx), "s"(tos[i]), "v"(p * 8), "s"(sb), "s"(tbs[i]) : "memory");
    }
    STAMP(2);
    bar();
    auto add4 = [](f4 a, f4 c) { return f4{sadd(a.x, c.x), sadd(a.y, c.y), sadd(a.z, c.z), sadd(a.w, c.w)}; };
    f4 p0[4], s0a;
#pragma unroll
    for (int v = 0; v < 4; ++v) p0[v] = *(LAS f4*)(S + prb + tps[0] + v * GW);
    LAST(13, acc[1], 0, C_ZERO); PUT(2, acc[0], 0); LAST(12, acc[1], 1, C_ACC); PUT(2, acc[0], 1);
    s0a = add4(add4(p0[0], p0[1]), add4(p0[2], p0[3])); GOT(s0a);
#pragma unroll
    for (int v = 0; v < 4; ++v) p0[v] = *(LAS f4*)(S + prb + tps[0] + (4 + v) * GW);
    LAST(11, acc[1], 2, C_ACC); PUT(2, acc[0], 2); LAST(10, acc[1], 3, C_ACC); PUT(2, acc[0], 3);
    s0a = add4(s0a, add4(add4(p0[0], p0[1]), add4(p0[2], p0[3]))); GOT(s0a);
    lds_put(CNT + 16 * 11 + 4 * w, 1);
    LAST(0, acc[1], 4, C_ACC); PUT(2, acc[0], 4);
    GOT(fh);
#pragma unroll
    for (int i = 0; i < 3; ++i) { GOT(xr[i]); GOT(bb[i]); }
    v4u yv[3];
    const bool y_early = __all(fh == tag);
    __builtin_amdgcn_fence(__ATOMIC_ACQUIRE, "wavefront");
    if (y_early) {
#pragma unroll
      for (int i = 0; i < 3; ++i) yv[i] = ld16(rws, ylb, tys[i]);
    }
    PIN();
    LAST(63, acc[1], 5, C_ACC); LAST(63, acc[1], 6, C_ACC);
    if (w < 4) STAMP(12);
    lds_wait_ge(S, CNT + 16 * 11 + 4 * (lane & 7), 1);
    asm volatile("s_nop 7" : "+v"(acc[1][0]), "+v"(acc[1][1]), "+v"(acc[1][2]), "+v"(acc[1][3]), "+v"(acc[1][4]));
#pragma unroll
    for (int n = 0; n < 5; ++n) PUT(0, acc[1], n);
    if (!y_early) {
#pragma unroll 1
      for (unsigned s = 0; s < SPIN_CAP; ++s) { if (__all(ld_word(ws, fy_off) == tag)) break; __builtin_amdgcn_s_sleep(1); }
      __builtin_amdgcn_fence(__ATOMIC_ACQUIRE, "wavefront");
#pragma unroll
      for (int i = 0; i < 3; ++i) yv[i] = ld16(rws, ylb, tys[i]);
    }
    bar();
    if (w < 4) STAMP(13);
    const __amdgpu_buffer_rsrc_t ro = __builtin_amdgcn_make_buffer_rsrc(out, 0, L * D * 2, 0x00020000);
    f4 pr[2][8];
#pragma unroll
    for (int v = 0; v < 16; ++v) pr[v >> 3][v & 7] = *(LAS f4*)(S + prb + tps[1 + (v >> 3)] + (v & 7) * GW);
    PIN();
    v2u yb[3];
#pragma unroll
    for (int i = 0; i < 3; ++i) {
      const f4 sa = i == 0 ? s0a : ((pr[i - 1][0] + pr[i - 1][1]) + (pr[i - 1][2] + pr[i - 1][3])) + ((pr[i - 1][4] + pr[i - 1][5]) + (pr[i - 1][6] + pr[i - 1][7]));
      yb[i] = v2u{pk2(unpk(pk2(sa.xy)) + unpk(bb[i].x)), pk2(unpk(pk2(sa.zw)) + unpk(bb[i].y))};
    }
#pragma unroll
    for (int i = 0; i < 3; ++i) {
      const unsigned e = (lane & 15) + tsel[i] - 8 * g, one = e < 8u ? 0x3f80u << (16 * (e & 1)) : 0u;
      const v4u sel = {(e & 6) == 0 ? one : 0u, (e & 6) == 2 ? one : 0u, (e & 6) == 4 ? one : 0u, (e & 6) == 6 ? one : 0u};
      const f4 yt = __builtin_amdgcn_mfma_f32_16x16x32_bf16(asbf(yv[i]), asbf(sel), f4{0.f, 0.f, 0.f, 0.f}, 0, 0, 0);
      const int from = ((lane >> 2) + 16 * p) * 4;
      const unsigned y0 = __builtin_amdgcn_ds_bpermute(from, pk2(yt.xy)), y1 = __builtin_amdgcn_ds_bpermute(from, pk2(yt.zw));
      __builtin_amdgcn_raw_buffer_store_b64(v2u{pk2(unpk(pk2(unpk(xr[i].x) + unpk(y0))) + unpk(yb[i].x)), pk2(unpk(pk2(unpk(xr[i].y) + unpk(y1))) + unpk(yb[i].y))}, ro,
                                            ovb + (i == 0 || (i == 1 ? w < 7 : w < 5) ? 0u : OOB), tos[i], 0);
    }
  }
  STAMP(10);
  STAMP_OUT();
  if (b == 0 && w == 7) {
#pragma unroll 1
    for (unsigned s = 0, j = 0; s < SPIN_CAP && j < 4; ++s)
      if (__all((unsigned)(ld_word(ws, WS_ST + (64 * j + lane) * 8) >> 32) == (unsigned)tag)) ++j;
    if (lane == 0) __hip_atomic_store((GAS u64*)ws, tag, __ATOMIC_RELAXED, __HIP_MEMORY_SCOPE_AGENT);
  }
  asm volatile("s4_code_end:");
}
}

extern "C" void kernel_launch(void* const* d_in, const int* in_sizes, int n_in, void* d_out, int out_size, void* d_ws, size_t ws_size,
                              hipStream_t stream) {
  (void)in_sizes; (void)n_in; (void)out_size; (void)ws_size;
  (void)hipFuncSetAttribute((const void*)s4mlp_kernel, hipFuncAttributeMaxDynamicSharedMemorySize, LDS_BYTES);
  s4mlp_kernel<<<256, 512, LDS_BYTES, stream>>>((const u16*)d_in[0], (const u16*)d_in[1], (const u16*)d_in[2], (const u16*)d_in[3], (const u16*)d_in[4],
                                                (const u16*)d_in[5], (const u16*)d_in[6], (const u16*)d_in[11], (const u16*)d_in[7], (const u16*)d_in[8],
                                                (const u16*)d_in[10], (const u16*)d_in[12], (const u16*)d_in[13], (u16*)d_out, (char*)d_ws);
}
```

```cpp
#include <hip/hip_runtime.h>
#include <hip/hip_bf16.h>
#include <stdint.h>

namespace {
typedef unsigned short u16;
typedef unsigned long long u64;
typedef __bf16 bf8 __attribute__((ext_vector_type(8)));
typedef __bf16 bf2 __attribute__((ext_vector_type(2)));
typedef short s4 __attribute__((ext_vector_type(4)));
typedef float f16v __attribute__((ext_vector_type(16)));
typedef float f4 __attribute__((ext_vector_type(4)));
typedef float f2 __attribute__((ext_vector_type(2)));
typedef unsigned v4u __attribute__((ext_vector_type(4)));
typedef unsigned v2u __attribute__((ext_vector_type(2)));
#define LAS __attribute__((address_space(3)))
#define GAS __attribute__((address_space(1)))
#define DI __device__ __forceinline__

constexpr int L = 2048, D = 640, N = 128, F = 1728;
constexpr unsigned WS_END0 = (1u << 20) + 168u * 54 * 1024 + 2 * 640u * 4096;
constexpr unsigned WS_FLAG = 4096;
constexpr int FL_H = 0, FL_X = 224, FL_Y = 384, FL_K = 608, FL_W = 640;
constexpr unsigned WS_ST = 256u << 10;
constexpr unsigned WS_CS = WS_ST + 2048 * 8;
constexpr unsigned WS_K3 = WS_END0;
constexpr unsigned WS_HT = 1u << 20;
constexpr unsigned WS_W2T = WS_HT + 128u * 54 * 1024;
constexpr unsigned WS_XT = WS_W2T + 40u * 54 * 1024;
constexpr unsigned WS_YT = WS_XT + 640u * 4096;
constexpr unsigned WS_END = WS_YT + 2 * 640u * 4096;
static_assert(WS_K3 == WS_YT + 640u * 4096, "workspace map");
constexpr unsigned WS_DBG = 32u << 20;
constexpr unsigned OOB = 0x40000000u;
constexpr int LDS_BYTES = 163840;
constexpr int SLOT = 32768;
constexpr int TAB = 98304, TROW = 272, TW = 32 * TROW;
constexpr int CNT = TAB + 4 * TW, TAGL = CNT + 240;
constexpr int COLT = CNT + 256;
constexpr int W2U_G = COLT + 2048, W2U_S = 16384;
constexpr int KCH = 4608, KARR = LDS_BYTES - 6 * KCH;
constexpr int UCP = 65536, UST = 4160, UCH = 4 * UST;
constexpr int GT = 1024, GW = 5 * GT, GBUF = 8 * GW;
static_assert(W2U_G + 8192 <= KARR + 3 * KCH && UCP + 3 * UCH <= CNT && 3 * GBUF <= CNT, "LDS map");
#define NB1 20
#define NB0 8
#define WARM 0
#define NA 0
#define CPRIO 0
#define FYJ 6
#define FXN 1
#define BPRIO 1
#define SKEW 1
#define MAP48 1
#define WARMA 1
#define WARMB 2
#define W7P 0
#define LDLY 0
#define LAUX 16
constexpr unsigned SPIN_CAP = 1u << 17;
#define SLP 1

DI float bflo(unsigned v) { return __uint_as_float(v << 16); }
DI float bfhi(unsigned v) { return __uint_as_float(v & 0xffff0000u); }
DI f2 unpk(unsigned v) { return f2{bflo(v), bfhi(v)}; }
DI unsigned pk2(f2 p) { return __builtin_bit_cast(unsigned, __builtin_convertvector(p, bf2)); }
DI unsigned pk(float a, float b) { return pk2(f2{a, b}); }
DI float rbf(float a) { return bflo(pk(a, a)); }
DI bf8 asbf(v4u v) { return __builtin_bit_cast(bf8, v); }
DI v4u mk_srd(const void* p, unsigned bytes) { u64 a = (u64)p; return v4u{(unsigned)a, (unsigned)(a >> 32) & 0xffffu, bytes, 0x00020000u}; }
#define SET_M0(v) asm volatile("s_mov_b32 m0, %0\n\ts_nop 0" ::"s"(__builtin_amdgcn_readfirstlane(v)) : "memory")
#define DMA(srd, voff, soff, imm) asm volatile("buffer_load_dwordx4 %0, %1, %2 offen offset:" #imm " lds" ::"v"(voff), "s"(srd), "s"(soff) : "memory")
#define WAITV(n) asm volatile("s_waitcnt vmcnt(" #n ")" ::: "memory")
#define PIN() __builtin_amdgcn_sched_barrier(0)
#define ORDER() asm volatile("" ::: "memory")
DI void bar() { asm volatile("s_waitcnt lgkmcnt(0)\n\ts_barrier" ::: "memory"); }
DI void bar_raw() { PIN(); asm volatile("s_barrier" ::: "memory"); PIN(); }
DI v2u trd(LAS char* p) { return __builtin_bit_cast(v2u, __builtin_amdgcn_ds_read_tr16_b64_v4i16((LAS s4*)p)); }
DI v4u trd2(LAS char* p0, LAS char* p1) { v2u a = trd(p0), b = trd(p1); return v4u{a.x, a.y, b.x, b.y}; }
DI float dot2(unsigned a, unsigned b, float c) { return __builtin_amdgcn_fdot2_f32_bf16(__builtin_bit_cast(bf2, a), __builtin_bit_cast(bf2, b), c, false); }
DI float dot8(v4u a, v4u b, float c) { return dot2(a.w, b.w, dot2(a.z, b.z, dot2(a.y, b.y, dot2(a.x, b.x, c)))); }
DI float sum8(v4u a, float c) { const unsigned o = 0x3f803f80u; return dot2(a.w, o, dot2(a.z, o, dot2(a.y, o, dot2(a.x, o, c)))); }
DI f2 fma2(f2 a, f2 b, f2 c) { return __builtin_elementwise_fma(a, b, c); }
struct cm { f2 p, n; };
DI cm mkm(f2 b) { return cm{b, f2{-b.y, b.x}}; }
DI f2 cmul(f2 a, cm b) { return fma2(a.yy, b.n, a.xx * b.p); }
DI f2 cexpl(float a, float th, float l) {
  const float m = __builtin_amdgcn_exp2f(a * l), rv = __builtin_amdgcn_fractf(th * l);
  return f2{m, m} * f2{__builtin_amdgcn_cosf(rv), __builtin_amdgcn_sinf(rv)};
}
DI float xor32(float v) { const auto r = __builtin_amdgcn_permlane32_swap(__float_as_uint(v), __float_as_uint(v), false, false); return __uint_as_float(r[0]) + __uint_as_float(r[1]); }
DI void st16(__amdgpu_buffer_rsrc_t r, v4u v, unsigned off) { __builtin_amdgcn_raw_buffer_store_b128(v, r, off, 0, 16); }
DI v4u ld16(__amdgpu_buffer_rsrc_t r, unsigned voff, unsigned soff) { return __builtin_amdgcn_raw_buffer_load_b128(r, voff, soff, LAUX); }
DI void put_flag(char* ws, int k, u64 tag) { __hip_atomic_store((GAS u64*)(ws + WS_FLAG + 128u * k), tag, __ATOMIC_RELAXED, __HIP_MEMORY_SCOPE_AGENT); }
DI u64 ld_word(char* ws, unsigned off) { return __hip_atomic_load((GAS u64*)(ws + off), __ATOMIC_RELAXED, __HIP_MEMORY_SCOPE_AGENT); }
DI void wait_flags(char* ws, int k, u64 tag) {
#pragma unroll 1
  for (unsigned s = 0; s < SPIN_CAP; ++s) {
    if (__all(ld_word(ws, WS_FLAG + 128u * k) == tag)) break;
    __builtin_amdgcn_s_sleep(1);
  }
  __builtin_amdgcn_fence(__ATOMIC_ACQUIRE, "wavefront");
}
DI u64 next_tag(u64 ep) { return ep * 6364136223846793005ull + 1442695040888963407ull; }
DI float ld_bf_uniform(const u16* p, int i) { const unsigned v = ((const unsigned*)p)[i >> 1]; return (i & 1) ? bfhi(v) : bflo(v); }
DI void lds_put(unsigned addr, unsigned v) { asm volatile("s_mov_b64 exec, 1\n\tds_write_b32 %0, %1\n\ts_mov_b64 exec, -1" ::"v"(addr), "v"(v) : "memory"); }
DI bool lds_arrive(LAS char* S, int c, int q) {
  asm volatile("s_waitcnt lgkmcnt(0)" ::: "memory");
  lds_put(CNT + 16 * c + 4 * q, 1u);
  const v4u v = *(volatile LAS v4u*)(S + CNT + 16 * c);
  return __builtin_amdgcn_readfirstlane(v.x & v.y & v.z & v.w);
}
DI void lds_wait(LAS char* S, int c) {
#pragma unroll 1
  for (unsigned s = 0; s < (SPIN_CAP << 4); ++s) {
    const v4u v = *(volatile LAS v4u*)(S + CNT + 16 * c);
    if (__builtin_amdgcn_readfirstlane(v.x & v.y & v.z & v.w)) break;
    __builtin_amdgcn_s_sleep(SLP);
  }
  ORDER();
}
DI void lds_set(LAS char* S, int off, unsigned v) { asm volatile("s_waitcnt lgkmcnt(0)" ::: "memory"); lds_put(off, v); }
DI unsigned lds_get(LAS char* S, unsigned off) { return *(volatile LAS unsigned*)(S + off); }
DI void lds_wait_ge(LAS char* S, unsigned off, unsigned n) {
#pragma unroll 1
  for (unsigned s = 0; s < (SPIN_CAP << 4); ++s) {
    if (__all(lds_get(S, off) >= n)) break;
    __builtin_amdgcn_s_sleep(SLP);
  }
  ORDER();
}
DI u64 rd_tag(LAS char* S) { const v2u t = *(volatile LAS v2u*)(S + TAGL); return ((u64)(unsigned)__builtin_amdgcn_readfirstlane(t.y) << 32) | (unsigned)__builtin_amdgcn_readfirstlane(t.x); }
DI u64 get_tag(LAS char* S) { lds_wait_ge(S, TAGL + 8, 1); return rd_tag(S); }
DI u64 ld_hidden(char* ws, unsigned off) { u64 v; asm volatile("global_load_dwordx2 %0, %1, %2 sc1" : "=v"(v) : "v"(off), "s"(ws) : "memory"); return v; }
DI float sadd(float a, float b) { float r; asm("v_add_f32 %0, %1, %2" : "=v"(r) : "v"(a), "v"(b)); return r; }
DI float smul(float a, float b) { float r; asm("v_mul_f32 %0, %1, %2" : "=v"(r) : "v"(a), "v"(b)); return r; }
DI float sfms(float a, float b, float c) { float r; asm("v_fma_f32 %0, %1, %2, -%3" : "=v"(r) : "v"(a), "v"(b), "v"(c)); return r; }
DI float ssub(float a, float b) { float r; asm("v_sub_f32 %0, %1, %2" : "=v"(r) : "v"(a), "v"(b)); return r; }
DI bool tag_ok(u64 g, u64 tag) { return __all((unsigned)(g >> 32) == (unsigned)tag); }
DI u64 wait_granule(char* ws, unsigned off, u64 tag) {
  u64 g = 0;
#pragma unroll 1
  for (unsigned s = 0; s < SPIN_CAP; ++s) { g = ld_word(ws, off); if (tag_ok(g, tag)) break; }
  return g;
}
DI void ld16_hidden(v4u& d, v4u srd, unsigned voff, unsigned soff) { asm volatile("buffer_load_dwordx4 %0, %1, %2, %3 offen sc1" : "=v"(d) : "v"(voff), "s"(srd), "s"(soff) : "memory"); }
#define GOT(v) asm volatile("" : "+v"(v))
DI void check_flags(char* ws, int k, u64 seen, u64 tag) {
  if (!__all(seen == tag)) wait_flags(ws, k, tag);
  __builtin_amdgcn_fence(__ATOMIC_ACQUIRE, "wavefront");
}
#define STAMP(k)
#define STAMP_DECL
#define STAMP_OUT()

__global__ __launch_bounds__(512, 2) void s4mlp_kernel(const u16* __restrict__ x, const u16* __restrict__ Are, const u16* __restrict__ Aim,
                                                      const u16* __restrict__ Bre, const u16* __restrict__ Bim, const u16* __restrict__ Cre,
                                                      const u16* __restrict__ Cim, const u16* __restrict__ b1, const u16* __restrict__ logdt,
                                                      const u16* __restrict__ dskip, const u16* __restrict__ w1, const u16* __restrict__ w2,
                                                      const u16* __restrict__ b2, u16* __restrict__ out, char* ws) {
  LAS char* const S = (LAS char*)0;
  const int tid = threadIdx.x, lane = tid & 63, w = __builtin_amdgcn_readfirstlane(tid >> 6), wq = w & 3;
  const int b = blockIdx.x;
  STAMP_DECL;
  STAMP(0);
  v4u pf0 = {}, pf1 = {};
  if (w == 7) {
    unsigned len;
    asm volatile("s4_code_begin:\n\ts_mov_b32 %0, s4_code_end-s4_code_begin-256" : "=s"(len));
    const v4u sc = mk_srd((const void*)__builtin_amdgcn_s_getpc(), __builtin_amdgcn_readfirstlane(len));
    asm volatile("s_nop 4\n\tbuffer_load_dwordx4 %0, %2, %3, 0 offen\n\tbuffer_load_dwordx4 %1, %4, %3, 0 offen"
                 : "=&v"(pf0), "=&v"(pf1) : "v"((b >> 3) * 1024 + lane * 16), "s"(sc), "v"((b >> 3) * 1024 + lane * 16 + 32768) : "memory");
    *(LAS unsigned*)(S + CNT + 4 * lane) = 0u;
    __builtin_amdgcn_s_setprio(W7P);
  }
  asm volatile("" ::"s"(x), "s"(w1), "s"(ws), "s"(Are), "s"(Aim), "s"(Bre), "s"(Bim), "s"(Cre), "s"(Cim), "s"(w2), "s"(logdt), "s"(dskip));
  bar();
#define IDS()                                                                                                              \
  int bl = b;                                                                                                              \
  asm volatile("" : "+s"(bl));                                                                                             \
  bl = __builtin_amdgcn_readfirstlane(bl);                                                                                 \
  const int xcd = bl & 7, bi = bl >> 3;                                                                                    \
  const int rp = 4 * (xcd >> 1) + (bi >> 3), cb = 7 * (xcd & 1) + (bi & 7);                                 \
  const bool spare = (bi & 7) == 7;                                                                       \
  const int c0 = cb * 128 > F - 128 ? F - 128 : cb * 128, gi = rp * 14 + cb, sp = xcd * 4 + (bi >> 3);                      \
  const int gq = gi / 28, gm = gi % 28;   \
  const int ch00 = spare ? 80 * (sp >> 2) + 56 + 6 * (sp & 3) : 80 * gq + 2 * gm, ch3 = 80 * gq + 56 + gm;                   \
  const int nbat = spare ? 3 : 1, nch = gm < 24 ? 3 : 2;                                                                   \
  const int rt = MAP48 ? 4 * xcd + (bi >> 3) : 8 * (xcd >> 1) + (bi >> 2), ct = MAP48 ? (bi & 7) : 4 * (xcd & 1) + (bi & 3);                                 \
  const int kar = spare ? KARR : KARR + 3 * KCH;                                                                             \
  const int s0 = SKEW == 0 ? 0 : SKEW == 1 ? (cb * 3 + rp * 5 + (rp >> 1)) % 10 : SKEW == 2 ? 5 * ((cb + rp) & 1) : (xcd * 5 + (xcd >> 1)) % 10;             \
  const unsigned w2s = WS_W2T + (ct * 270 + w) * 1024;                                                                      \
  const int fhk = FL_H + (rt >> 1) * 14 + (lane < 14 ? lane : 0);                                                          \
  const unsigned hb0 = WS_HT + ((rt * 4 + (ct & 3)) * 54 + w) * 1024;                                                      \
  (void)c0; (void)ch3; (void)nch; (void)ch00; (void)nbat; (void)kar; (void)s0; (void)w2s; (void)fhk; (void)hb0
  const __amdgpu_buffer_rsrc_t rws = __builtin_amdgcn_make_buffer_rsrc(ws, 0, WS_END, 0x00020000);
  const int h = lane >> 5, g = lane >> 4, q = (lane & 15) >> 2, p = lane & 3, blk = g & 1;
  auto fw_flag = [&](int ct) { const int k = (lane >> 1) * 8 + w; return FL_W + (k < 54 ? k : w) * 10 + ((ct * 80 + (lane & 1) * 79) >> 6); };
  const unsigned k6 = w < 6 ? 49152u : 0u;
  v4u wf2[35];
  const unsigned w2v = lane * 16, w2v6 = w2v + (w < 6 ? 0u : OOB);
  auto load_w2_ = [&](unsigned w2s, int i0, int i1) {
#pragma unroll
    for (int i = 0; i < 35; ++i)
      if (i >= i0 && i < i1) wf2[i] = ld16(rws, i >= 30 ? w2v6 : w2v, w2s + (i % 5) * (54 * 1024) + (i / 5) * 8192);
  };
  auto warm_w2 = [&](int c2, u64 tag) {
    wait_flags(ws, fw_flag(c2), tag);
    load_w2_(WS_W2T + (c2 * 270 + w) * 1024, 0, 35);
#pragma unroll
    for (int i = 0; i < 35; ++i) GOT(wf2[i]);
  };
  auto settle_w2 = [&](int n) {
#pragma unroll
    for (int i = 0; i < 35; ++i) if (i < n) GOT(wf2[i]);
  };
  v4u hf[7], junk = {};
  auto load_h = [&](unsigned base) {
#pragma unroll
    for (int i = 0; i < 7; ++i) ld16_hidden(hf[i], mk_srd(ws, WS_END), w2v, base + (i < 6 ? i * 8192u : k6));
  };
  unsigned tps[3], tos[3], tys[3], tbs[3], hb1, hb2, hb3, tsel[3], ovb, prb, ylb, fy_off;
  LAS char* Pw;
  auto setup_c = [&](int rt, int ct) {
#pragma unroll
    for (int i = 0; i < 3; ++i) {
      const int id0 = i == 0 ? w : i == 1 ? w + 8 : w + 15, id = id0 < 20 ? id0 : 0, pt = id / 5, n = id % 5, t0 = ((pt + ct) & 3) * 16;
      tps[i] = (pt == 3 ? 0 : pt) * GBUF + n * GT;
      tos[i] = ((rt * 64 + t0) * D + ct * 80 + 16 * n) * 2;
      tys[i] = WS_YT + (ct * 80 + 16 * n) * 4096 + rt * 128 + (t0 >> 5) * 64;
      tsel[i] = t0 & 16;
      tbs[i] = (ct * 80 + 16 * n) * 2;
    }
    hb1 = WS_HT + ((rt * 4 + ((ct + 1) & 3)) * 54 + w) * 1024; hb2 = WS_HT + ((rt * 4 + ((ct + 2) & 3)) * 54 + w) * 1024; hb3 = WS_HT + ((rt * 4 + ((ct + 3) & 3)) * 54 + w) * 1024;
    asm volatile("" ::"s"(tps[0]), "s"(tps[1]), "s"(tps[2]), "s"(tos[0]), "s"(tos[1]), "s"(tos[2]), "s"(tys[0]), "s"(tys[1]), "s"(tys[2]), "s"(tbs[0]), "s"(tbs[1]), "s"(tbs[2]),
                 "s"(hb1), "s"(hb2), "s"(hb3), "s"(tsel[0]), "s"(tsel[1]), "s"(tsel[2]));
    ovb = ((lane >> 2) * D + 4 * p) * 2;
    prb = p * 256 + (((lane >> 2) + 4 * p) & 15) * 16;
    ylb = (lane & 15) * 4096 + g * 16;
    fy_off = WS_FLAG + 128u * (FL_Y + ct * 28 + (lane < 28 ? lane : 0));
    unsigned pw_ = w * GW + g * 256 + (((lane & 15) + 4 * g) & 15) * 16;
    GOT(ovb); GOT(prb); GOT(ylb); GOT(fy_off); GOT(pw_);
    Pw = S + pw_;
    PIN();
  };
  u64 tag, fh = 0;
  if (w >= 4) {
    IDS();
    int sn = s0;
    const int u0 = spare ? 448 + 3 * sp : 2 * gi;
    const v4u sx = mk_srd(x, L * D * 2), sw1 = mk_srd(w1, D * F * 2);
    unsigned vx[4], vw[4];
    auto ring_setup = [&]() {
      int ln = lane;
      asm volatile("" : "+v"(ln));
#pragma unroll
      for (int j = 0; j < 4; ++j) {
        const int rx = 8 * j + (ln >> 3);
        vx[j] = (rp * 128 + rx) * (D * 2) + 16 * ((ln & 7) ^ ((rx >> 1) & 7)) - 1024 * j;
        vw[j] = (4 * j + (ln >> 4)) * (F * 2) + c0 * 2 + 16 * ((ln & 15) ^ (((ln >> 4) << 2) | j)) - 1024 * j;
      }
    };
    auto quarter = [&](int st, int slot, int k) {
      const unsigned so = st * 128 + k * (32 * D * 2), sv = st * (64 * F * 2) + k * (16 * F * 2);
      SET_M0(slot * SLOT + k * 4096);
      DMA(sx, vx[0], so, 0); DMA(sx, vx[1], so, 1024); DMA(sx, vx[2], so, 2048); DMA(sx, vx[3], so, 3072);
      SET_M0(slot * SLOT + 16384 + k * 4096);
      DMA(sw1, vw[0], sv, 0); DMA(sw1, vw[1], sv, 1024); DMA(sw1, vw[2], sv, 2048); DMA(sw1, vw[3], sv, 3072);
    };
    if (w == 7) {
      const __amdgpu_buffer_rsrc_t rx = __builtin_amdgcn_make_buffer_rsrc((void*)x, 0, L * D * 2, 0x00020000),
                                   rw2 = __builtin_amdgcn_make_buffer_rsrc((void*)w2, 0, D * F * 2, 0x00020000);
      const u64 ep = ld_word(ws, 0);
      v4u xv[10], wv[3][4];
#pragma unroll
      for (int i = 0; i < 10; ++i) xv[i] = __builtin_amdgcn_raw_buffer_load_b128(rx, (lane >> 3) * (D * 2) + (lane & 7) * 16 + i * 128, b * (8 * D * 2), 0);
#pragma unroll
      for (int k = 0; k < 3; ++k) {
        const int u = u0 + k;
        const unsigned vo = (lane >> 3) * (D * 2) + (lane & 7) * 16 + ((k < 2 || spare) && u < 540 ? 0u : OOB);
#pragma unroll
        for (int i = 0; i < 4; ++i) wv[k][i] = __builtin_amdgcn_raw_buffer_load_b128(rw2, vo, ((u / 10) * 32 + 8 * i) * (D * 2) + (u % 10) * 128, 0);
      }
      PIN();
      __builtin_amdgcn_s_setprio(0);
      ring_setup();
      const u64 tg = next_tag(ep);
      lds_put(TAGL, (unsigned)tg); lds_put(TAGL + 4, (unsigned)(tg >> 32));
      lds_set(S, TAGL + 8, 1);
      GOT(pf0); GOT(pf1);
      float sm[2] = {}, sq[2] = {};
#pragma unroll
      for (int i = 0; i < 10; ++i) { sm[i & 1] = sum8(xv[i], sm[i & 1]); sq[i & 1] = dot8(xv[i], xv[i], sq[i & 1]); }
      float sum = sm[0] + sm[1], ssq = sq[0] + sq[1];
#pragma unroll
      for (int m = 1; m < 8; m <<= 1) { sum += __shfl_xor(sum, m); ssq += __shfl_xor(ssq, m); }
      const float mean = sum * (1.f / D), var = fmaxf(ssq * (1.f / D) - mean * mean, 0.f);
      const unsigned ms = pk(mean, __builtin_amdgcn_sqrtf(rbf(rbf(var) + 1e-5f)));
      if ((lane & 7) == 0) __hip_atomic_store((GAS u64*)(ws + WS_ST + (lane >> 3) * 2048 + b * 8), (tg << 32) | ms, __ATOMIC_RELAXED, __HIP_MEMORY_SCOPE_AGENT);
      STAMP(1);
      LAS char* U = S + (spare ? W2U_S : W2U_G);
      LAS char* B = U + (4 * (blk + 4 * (g >> 1)) + q) * 128 + 8 * p;
      GOT(wv[2][0]); GOT(wv[2][1]); GOT(wv[2][2]); GOT(wv[2][3]);
#pragma unroll
      for (int k = 0; k < 3; ++k) {
        const int u = u0 + k;
        if ((k < 2 || spare) && u < 540) {
#pragma unroll
          for (int i = 0; i < 4; ++i) *(LAS v4u*)(U + k * 4096 + i * 1024 + lane * 16) = wv[k][i];
#pragma unroll
          for (int n = 0; n < 4; ++n) st16(rws, trd2(B + k * 4096 + 32 * n, B + k * 4096 + 32 * n + 1024), lane * 16 + WS_W2T + (((u % 10) * 4 + n) * 54 + u / 10) * 1024);
        }
      }
    } else if (!spare) {
      ring_setup();
      if (LDLY) __builtin_amdgcn_s_sleep(LDLY);
#pragma unroll 1
      for (int t = wq; t < 12; t += 3) {
        if (t >= 9) { WAITV(8); lds_put(CNT + 28, 1); lds_put(CNT + 16 + 4 * wq, 1); }
        quarter(sn + (t >> 2) >= 10 ? sn + (t >> 2) - 10 : sn + (t >> 2), t >> 2, t & 3);
      }
      WAITV(8);
      STAMP(1);
    } else if (sp < 27) {
      const __amdgpu_buffer_rsrc_t rw1 = __builtin_amdgcn_make_buffer_rsrc((void*)w1, 0, D * F * 2, 0x00020000);
      v4u v[27];
#pragma unroll
      for (int i = 0; i < 27; ++i) v[i] = __builtin_amdgcn_raw_buffer_load_b128(rw1, (216 * wq + 8 * i + (lane >> 3)) * (F * 2) + (lane & 7) * 16, sp * 128, 0);
      PIN();
      f2 a[4] = {};
#pragma unroll
      for (int i = 0; i < 27; ++i)
#pragma unroll
        for (int e = 0; e < 4; ++e) a[e] += unpk(v[i][e]);
      *(LAS f4*)(S + (wq * 64 + lane) * 32) = f4{a[0].x, a[0].y, a[1].x, a[1].y};
      *(LAS f4*)(S + (wq * 64 + lane) * 32 + 16) = f4{a[2].x, a[2].y, a[3].x, a[3].y};
      lds_put(CNT + 16 + 12, 1);
      if (lds_arrive(S, 1, wq)) {
        float t = 0.f;
#pragma unroll
        for (int i = 0; i < 24; ++i) t += *(LAS float*)(S + i * 256 + lane * 4);
        __hip_atomic_store((GAS u64*)(ws + WS_CS + (sp * 64 + lane) * 8), (get_tag(S) << 32) | __float_as_uint(t), __ATOMIC_RELAXED, __HIP_MEMORY_SCOPE_AGENT);
      }
    }
    int fwk = fw_flag(ct);
    GOT(fwk);
    STAMP(2);
    if (spare) {
      tag = get_tag(S);
      if (w == 7) {
        WAITV(0);
        if (lane < 3 && u0 + lane < 540) put_flag(ws, FL_W + u0 + lane, tag);
      }
      if (WARMB) warm_w2(2 * (bi >> 3), tag);
      if (WARMB > 1 && (bi >> 3) != 3) warm_w2(2 * (bi >> 3) + 1, tag);
      wait_flags(ws, fwk, tag);
      load_w2_(w2s, 0, 35);
      settle_w2(35);
      wait_flags(ws, fhk, tag);
      load_h(hb0);
    } else {
      const int t4 = tid - 256;
      const int fxk = FL_X + (lane & 15) * 10 + ((g == 2 && nch == 3 ? ch3 : ch00 + (g == 1)) >> 6);
      bar();
      STAMP(6);
      tag = rd_tag(S);
      sn = sn >= 7 ? sn - 7 : sn + 3;
      auto next = [&](int slot) { quarter(sn, slot, wq); sn = sn == 9 ? 0 : sn + 1; };
      next(3);
      WAITV(8);
      if (w == 7 && lane < 2) put_flag(ws, FL_W + u0 + lane, tag);
      bar_raw();
#pragma unroll 1
      for (int n = 0; n < 4; ++n) {
        next(n);
        WAITV(8);
        bar_raw();
      }
      next(0);
      u64 px = ld_hidden(ws, WS_FLAG + 128u * fxk);
      WAITV(9);
      bar_raw();
      STAMP(3);
      next(1);
      WAITV(8);
      bar_raw();
      STAMP(4);
      GOT(px);
      check_flags(ws, fxk, px, tag);
      v2u gr[8];
      v4u un[3];
#pragma unroll
      for (int i = 0; i < 8; ++i) gr[i] = __builtin_amdgcn_raw_buffer_load_b64(rws, t4 * 8, WS_ST + i * 2048, 16);
#pragma unroll
      for (int j = 0; j < 3; ++j) un[j] = ld16(rws, t4 * 16 + (j < nch ? 0u : OOB), WS_XT + (j < 2 ? ch00 + j : ch3) * 4096);
      u64 pw = ld_hidden(ws, WS_FLAG + 128u * fwk);
      WAITV(12);
      bar_raw();
      STAMP(5);
      __builtin_amdgcn_s_setprio(BPRIO);
      {
        unsigned bad = 0;
#pragma unroll
        for (int i = 0; i < 8; ++i) bad |= gr[i].y ^ (unsigned)tag;
        if (!__all(bad == 0)) {
#pragma unroll
          for (int i = 0; i < 8; ++i) gr[i].x = (unsigned)wait_granule(ws, WS_ST + i * 2048 + t4 * 8, tag);
        }
        float rr[8];
#pragma unroll
        for (int e = 0; e < 8; ++e) rr[e] = __builtin_amdgcn_rcpf(bfhi(gr[e].x));
        asm volatile("s_waitcnt vmcnt(0)" : "+v"(rr[0]), "+v"(rr[1]), "+v"(rr[2]), "+v"(rr[3]), "+v"(rr[4]), "+v"(rr[5]), "+v"(rr[6]), "+v"(rr[7]), "+v"(pw), "+v"(un[0]), "+v"(un[1]), "+v"(un[2])::"memory");
        check_flags(ws, fwk, pw, tag);
        load_w2_(w2s, 0, NB0);
        PIN();
        STAMP(1);
#pragma unroll
        for (int e = 0; e < 4; ++e) {
          const float n0 = smul(bflo(gr[2 * e].x), rr[2 * e]), n1 = smul(bflo(gr[2 * e + 1].x), rr[2 * e + 1]);
#pragma unroll
          for (int j = 0; j < 3; ++j) un[j][e] = pk(sfms(bflo(un[j][e]), rr[2 * e], n0), sfms(bfhi(un[j][e]), rr[2 * e + 1], n1));
        }
        LAS char* U = S + UCP + 32 + t4 * 16;
        if (t4 < 56) *(LAS v4u*)(S + (t4 < 24 ? UCP + (t4 >> 1) * UST + (t4 & 1) * 16 : KARR + 5 * KCH + (t4 - 24) * 16)) = v4u{0u, 0u, 0u, 0u};
#pragma unroll
        for (int j = 0; j < 3; ++j) *(LAS v4u*)(U + j * UCH) = un[j];
        lds_put(CNT + 16 * 7 + 4 * wq, 1);
        lds_wait(S, 7);
#pragma unroll
        for (int j = 0; j < 3; ++j) {
          const v4u d = un[j]; const v2u pv = *(LAS v2u*)(U + j * UCH - 8);
          const unsigned a0 = __builtin_amdgcn_alignbit(pv.y, pv.x, 16), a1 = __builtin_amdgcn_alignbit(d.x, pv.y, 16), a2 = __builtin_amdgcn_alignbit(d.y, d.x, 16),
                         a3 = __builtin_amdgcn_alignbit(d.z, d.y, 16), a4 = __builtin_amdgcn_alignbit(d.w, d.z, 16);
          *(LAS v4u*)(U + j * UCH + UST) = v4u{a1, a2, a3, a4};
          *(LAS v4u*)(U + j * UCH + 2 * UST) = v4u{pv.y, d.x, d.y, d.z};
          *(LAS v4u*)(U + j * UCH + 3 * UST) = v4u{a0, a1, a2, a3};
        }
      }
      if (nch == 3) wait_flags(ws, FL_K + 4 * gq + gm / 6, tag);
      const v4u k3 = ld16(rws, t4 * 16, WS_K3 + ch3 * 4096);
      ORDER();
      load_w2_(w2s, NB0, NB1);
      *(LAS v4u*)(S + KARR + 5 * KCH + 512 + t4 * 16) = k3;
      lds_put(CNT + 16 * 8 + 4 * wq, 1);
      STAMP(12);
      LAS char* Kp = S + kar + 512 + 32 * ((lane & 15) - (g >> 1)) + 16 * blk - 64 * wq - 256;
      LAS char* Up = S + UCP + q * UST + 32 + 32 * (g >> 1) - 16 * blk + 8 * p + 64 * wq - 8;
      LAS char* const Pp0 = S + wq * 8192 + g * 256 + (((lane & 15) ^ (2 * g)) * 16);
      lds_wait(S, 8);
      v4u kf[8], kg[8], ua[2], ub[2];
      auto rd_u = [&](int o, int s) {
        ua[s & 1] = trd2(Up + o * UCH + 512 * s + 8, Up + o * UCH + 512 * s); ub[s & 1] = trd2(Up + o * UCH + 512 * (7 - s) + 264, Up + o * UCH + 512 * (7 - s) + 256);
        kg[s] = *(LAS v4u*)(Kp + o * KCH + 512 * s);
      };
      rd_u(0, 0);
#pragma unroll
      for (int e = 7; e >= 0; --e) kf[e] = *(LAS v4u*)(Kp + 512 * e + 256);
      __builtin_amdgcn_s_setprio(1);
      lds_wait(S, 6);
      STAMP(13);
      auto channel = [&](int cj, bool last) {
        f4 acc[8];
        PIN();
#pragma unroll
        for (int s = 0; s < 8; ++s) {
          if (s < 7) rd_u(0, s + 1);
          if (last) {
#pragma unroll
            for (int i = NB1 + 2 * s; i < NB1 + 2 * s + 2 && i < 35; ++i) ld16_hidden(wf2[i], mk_srd(ws, WS_END), i >= 30 ? w2v6 : w2v, w2s + (i % 5) * (54 * 1024) + (i / 5) * 8192);
          }
          else if (s > 0) kf[8 - s] = *(LAS v4u*)(Kp + KCH + 512 * (8 - s) + 256);
#pragma unroll
          for (int R = 7; R >= s; --R) acc[R] = __builtin_amdgcn_mfma_f32_16x16x32_bf16(asbf(ua[s & 1]), asbf(kf[R - s]), s ? acc[R] : f4{0.f, 0.f, 0.f, 0.f}, 0, 0, 0);
#pragma unroll
          for (int R = 7 - s; R < 8; ++R) acc[R] = __builtin_amdgcn_mfma_f32_16x16x32_bf16(asbf(ub[s & 1]), asbf(kg[R - 7 + s]), acc[R], 0, 0, 0);
#pragma unroll
          for (int i = 0; i < 7; ++i) { __builtin_amdgcn_sched_group_barrier(0x8, 1, 0); __builtin_amdgcn_sched_group_barrier(0x90, 1, 0); }
          PIN();
        }
        if (!last) {
          kf[0] = *(LAS v4u*)(Kp + KCH + 256);
          rd_u(1, 0);
        } else {
          WAITV(15);
          static_assert(NB1 == 20, "the wait above counts the loads behind the flag's");
          GOT(fh);
          check_flags(ws, fhk, fh, tag);
          load_h(hb0);
          if (cj == 2) lds_wait_ge(S, CNT + 16 * 9 + 4 * p, 1);
        }
        PIN();
#pragma unroll
        for (int R = 0; R < 8; ++R) *(LAS f4*)(Pp0 + (cj & 1) * SLOT + R * 1024) = acc[R];
        lds_put(CNT + 16 * 2 + 4 * wq, cj + 1);
        Kp += KCH; Up += UCH;
      };
#pragma unroll 1
      for (int cj = 0; cj < nch - 1; ++cj) channel(cj, false);
      settle_w2(NB1);
      fh = ld_hidden(ws, WS_FLAG + 128u * fhk);
      channel(nch - 1, true);
    }
    STAMP(7);
    __builtin_amdgcn_s_setprio(CPRIO);
  } else {
    IDS();
    const int wf = w >> 1, tg = w & 1;
    unsigned xa[4], wa[2][2], pn[6];
    unsigned lgn, dskn;
    auto fetch = [&](int bt) {
      const int ch = ch00 + 2 * bt + wf, o = ch * N + tg * 64 + lane;
      pn[0] = Are[o]; pn[1] = Aim[o]; pn[2] = Bre[o]; pn[3] = Bim[o]; pn[4] = Cre[o]; pn[5] = Cim[o];
      lgn = logdt[ch]; dskn = dskip[ch];
      PIN();
    };
    fetch(0);
    int fwk = fw_flag(ct);
    GOT(fwk);
#pragma unroll
    for (int i = 0; i < 4; ++i) {
      const int row = 64 * tg + (lane & 31), c = 2 * wf + (i >> 1), t2 = i & 1;
      xa[i] = (row * 128 + 16 * (h ^ ((row >> 1) & 7))) ^ (32 * i);
      wa[i >> 1][i & 1] = 16384 + 256 * (8 * h + 4 * t2 + q) + 64 * (c ^ q) + 16 * ((2 * blk + (p >> 1)) ^ (2 * h + t2)) + 8 * (p & 1);
      GOT(xa[i]); GOT(wa[i >> 1][i & 1]);
    }
    PIN();
#pragma unroll 1
    for (int bt = 0; bt < nbat; ++bt) {
      const unsigned p0 = pn[0], p1 = pn[1], p2 = pn[2], p3 = pn[3], p4 = pn[4], p5 = pn[5];
      const float lg = bflo(lgn), dsk = bflo(dskn);
      if (bt + 1 < nbat) fetch(bt + 1);
      if (bt) lds_wait_ge(S, CNT + 16 * 5 + 4 * (w ^ 1), bt);
      LAS unsigned* T = (LAS unsigned*)(S + TAB + w * TW) + lane;
      LAS char* Tr = S + TAB + w * TW + g * 16 + (lane & 15) * TROW;
      constexpr int R = TROW / 4;
      const float dt = rbf(__expf(lg)), a = dt * bflo(p0) * 1.4426950408889634f;
      float th = dt * bflo(p1) * 0.15915494309189535f;
      th -= __builtin_rintf(th);
      const f2 e1 = cexpl(a, th, 1.f), e128 = cexpl(a, th, 128.f), e8 = cexpl(a, -th, 8.f);
      const cm m1 = mkm(e1), m128 = mkm(e128), m8 = mkm(e8), m16 = mkm(cmul(e8, m8));
      const f2 e2 = cmul(e1, m1);
      const cm m2 = mkm(e2), m4 = mkm(cmul(e2, m2));
      f2 y = cmul(f2{bflo(p4), -bflo(p5)}, mkm(f2{dt, dt} * f2{bflo(p2), -bflo(p3)})), y2 = cmul(y, m8), t = cexpl(a, th, 1024.f);
#pragma unroll
      for (int i = 0; i < 8; ++i) {
        T[(2 * i) * R] = pk2(y); T[(2 * i + 1) * R] = pk2(y2); T[(16 + i) * R] = pk2(t);
        if (i < 7) { y = cmul(y, m16); y2 = cmul(y2, m16); }
        t = cmul(t, m128);
      }
      T[24 * R] = pk2(t);
      v4u fy[4], fa[4][2];
      f4 ka[3] = {};
#pragma unroll
      for (int ks = 0; ks < 4; ++ks) { fy[ks] = *(LAS v4u*)(Tr + ks * 64); fa[ks][0] = *(LAS v4u*)(Tr + 16 * TROW + ks * 64); }
      PIN();
      f2 u = {1.f, 0.f}, v = e128, z = cmul(e128, m128);
#pragma unroll
      for (int i = 0; i < 8; ++i) {
        T[i * R] = pk2(u); T[(8 + i) * R] = pk2(v);
        if (i < 7) { u = cmul(u, m1); v = cmul(v, m1); }
        if (i < 2) {
          const f2 z2 = cmul(z, m2);
          T[(16 + 4 * i) * R] = pk2(z); T[(17 + 4 * i) * R] = pk2(z2); T[(18 + 4 * i) * R] = pk2(cmul(z, m4)); T[(19 + 4 * i) * R] = pk2(cmul(z2, m4));
        } else if (i < 6) {
          T[(20 + 2 * i) * R] = pk2(z); T[(21 + 2 * i) * R] = pk2(cmul(z, m4));
        }
        if (i < 5) z = cmul(z, m128);
      }
      PIN();
#pragma unroll
      for (int ks = 0; ks < 4; ++ks) ka[2] = __builtin_amdgcn_mfma_f32_16x16x32_bf16(asbf(fa[ks][0]), asbf(fy[ks]), ka[2], 0, 0, 0);
#pragma unroll
      for (int ks = 0; ks < 4; ++ks) { fa[ks][0] = *(LAS v4u*)(Tr + ks * 64); fa[ks][1] = *(LAS v4u*)(Tr + 16 * TROW + ks * 64); }
      PIN();
#pragma unroll
      for (int ks = 0; ks < 4; ++ks)
#pragma unroll
        for (int i = 0; i < 2; ++i) ka[i] = __builtin_amdgcn_mfma_f32_16x16x32_bf16(asbf(fa[ks][i]), asbf(fy[ks]), ka[i], 0, 0, 0);
      if (bt == 0) STAMP(1);
      {
        LAS char* X = S + TAB + w * TW + 4608 + lane * 16;
        *(LAS f4*)X = tg ? ka[0] : ka[1];
        *(LAS f4*)(X + 1024) = ka[2];
        lds_put(CNT + 16 * 4 + 4 * w, bt + 1);
        lds_wait_ge(S, CNT + 16 * 4 + 4 * (w ^ 1), bt + 1);
        LAS char* Y = S + TAB + (w ^ 1) * TW + 4608 + lane * 16;
        f4 km = (tg ? ka[1] : ka[0]) + *(LAS f4*)Y, k2 = ka[2] + *(LAS f4*)(Y + 1024);
        GOT(km); GOT(k2);
        lds_put(CNT + 16 * 5 + 4 * w, bt + 1);
        const int c = lane & 15;
        LAS char* K = S + kar + (2 * bt + wf) * KCH;
        LAS u16* T1 = (LAS u16*)(S + TAB + w * TW);
        const unsigned q01 = pk2(k2.xy), q23 = pk2(k2.zw);
        if (!tg) {
          if (lane == 0) km.x += dsk;
          *(LAS v2u*)(K + 512 + 256 * (g >> 1) + 16 * c + 8 * blk) = v2u{pk2(km.xy), pk2(km.zw)};
          LAS u16* T3 = T1 + 64 * g + c;
          T3[0] = (u16)q01; T3[16] = (u16)(q01 >> 16); T3[32] = (u16)q23; T3[48] = (u16)(q23 >> 16);
          ORDER();
          const unsigned c0_ = T1[lane], c1 = T1[1 + lane], d0 = T1[64 + lane], d1 = T1[65 + lane];
          if (h) *(LAS v4u*)(K + 16 * (lane & 31)) = v4u{0u, 0u, 0u, 0u};
          *(LAS v4u*)(K + 2560 + 16 * lane) = v4u{c0_ * 0x10001u, c0_ * 0x10001u, ((lane & 1) ? c1 : c0_) | (c1 << 16), c1 * 0x10001u};
          *(LAS v4u*)(K + 3584 + 16 * lane) = v4u{d0 * 0x10001u, d0 * 0x10001u, ((lane & 1) ? d1 : d0) | (d1 << 16), d1 * 0x10001u};
        } else {
          const unsigned p01 = pk2(km.xy), p23 = pk2(km.zw);
          if (g < 2) *(LAS v2u*)(T1 + 64 * g + 4 * c) = v2u{p01, p23};
          else { *(LAS unsigned*)(T1 + 64 * g + 2 * c) = p01; *(LAS unsigned*)(T1 + 64 * g + 32 + 2 * c) = p23; }
          if (lane == 0) T1[256] = (u16)q01;
          ORDER();
          const v2u a1 = *(LAS v2u*)(T1 + 4 * (lane & 31));
          const unsigned a4 = T1[4 * (lane & 31) + 4], b0 = *(LAS unsigned*)(T1 + 128 + 2 * lane), b2_ = T1[130 + 2 * lane];
          if (!h) *(LAS v4u*)(K + 1024 + 16 * lane) = v4u{(a1.x & 0xffffu) * 0x10001u, (a1.x >> 16) | (a1.y << 16), (a1.y & 0xffffu) * 0x10001u, (a1.y >> 16) | (a4 << 16)};
          *(LAS v4u*)(K + 1536 + 16 * lane) = v4u{(b0 & 0xffffu) * 0x10001u, b0, (b0 >> 16) * 0x10001u, b2_ * 0x10001u};
        }
      }
    }
    if (cb < 10 && !spare) {
      lds_wait(S, 1);
      v2u xt[8];
#pragma unroll
      for (int m = 0; m < 8; ++m) {
        const int rr = 32 * g + 4 * m + q;
        xt[m] = trd(S + 128 * rr + 16 * ((2 * w + (p >> 1)) ^ ((rr >> 1) & 7)) + 8 * (p & 1));
      }
      LAS char* X = S + TAB + w * TW;
#pragma unroll
      for (int k = 0; k < 4; ++k) *(LAS v4u*)(X + (lane & 15) * 272 + g * 64 + k * 16) = v4u{xt[2 * k].x, xt[2 * k].y, xt[2 * k + 1].x, xt[2 * k + 1].y};
#pragma unroll
      for (int k = 0; k < 4; ++k)
        st16(rws, *(LAS v4u*)(X + (4 * k + g) * 272 + (lane & 15) * 16), WS_XT + (s0 * 64 + 16 * w + 4 * k + g) * 4096 + rp * 256 + (lane & 15) * 16);
    }
    STAMP(2);
    if (!spare) {
      f16v acc[2][2] = {};
      v4u fx[4][2], fw[4][2];
      auto rd = [&](int set, int so, int ks) {
        fw[set][0] = trd2(S + wa[0][0] + so + 4096 * ks, S + wa[0][1] + so + 4096 * ks);
        fx[set][0] = *(LAS v4u*)(S + xa[ks] + so);
        fx[set][1] = *(LAS v4u*)(S + xa[ks] + so + 4096);
        fw[set][1] = trd2(S + wa[1][0] + so + 4096 * ks, S + wa[1][1] + so + 4096 * ks);
      };
      auto mm = [&](int set) {
#pragma unroll
        for (int i = 0; i < 4; ++i) acc[i >> 1][i & 1] = __builtin_amdgcn_mfma_f32_32x32x16_bf16(asbf(fw[set][i >> 1]), asbf(fx[set][i & 1]), acc[i >> 1][i & 1], 0, 0, 0);
#pragma unroll
        for (int i = 0; i < 3; ++i) { __builtin_amdgcn_sched_group_barrier(0x8, 1, 0); __builtin_amdgcn_sched_group_barrier(0x100, 2, 0); }
        __builtin_amdgcn_sched_group_barrier(0x8, 1, 0);
        PIN();
      };
      u64 fw_seen = 0, g0 = 0, g1 = 0, gc = 0;
      unsigned b1v = 0;
      const unsigned o0 = WS_ST + (lane & 7) * 2048 + (rp * 16 + 8 * tg + ((lane & 31) >> 3)) * 8, oc = WS_CS + (c0 + 64 * wf + lane) * 8;
      asm volatile("" : "+v"(acc[0][0]), "+v"(acc[0][1]), "+v"(acc[1][0]), "+v"(acc[1][1]));
      bar();
      STAMP(6);
      tag = rd_tag(S);
      rd(0, 0, 0); rd(1, 0, 1);
      PIN();
      auto flip = [&]() {
#pragma unroll
        for (int i = 0; i < 4; ++i) { xa[i] ^= 2 * SLOT; wa[i >> 1][i & 1] ^= 2 * SLOT; }
      };
#pragma unroll 1
      for (int n = 0; n < 4; ++n) {
        if (n == 3) { fw_seen = ld_hidden(ws, WS_FLAG + 128u * fwk); g0 = ld_hidden(ws, o0); g1 = ld_hidden(ws, o0 + 32); gc = ld_hidden(ws, oc);
          asm volatile("global_load_ushort %0, %1, %2" : "=v"(b1v) : "v"((c0 + 64 * wf + lane) * 2), "s"(b1) : "memory"); }
        rd(2, 0, 2); mm(0);
        rd(3, 0, 3); mm(1);
        rd(0, SLOT, 0); mm(2);
        rd(1, SLOT, 1); mm(3);
        if (n == FXN) WAITV(0);
        bar_raw();
        if (n == FXN && w == 1 && cb < 10 && lane == 0) put_flag(ws, FL_X + rp * 10 + (SKEW == 1 ? s0 : cb), tag);
        rd(2, SLOT, 2); mm(0);
        rd(3, SLOT, 3); mm(1);
        flip();
        rd(0, 0, 0); mm(2);
        rd(1, 0, 1); mm(3);
        bar_raw();
      }
      WAITV(0); GOT(fw_seen); GOT(g0); GOT(g1); GOT(gc); GOT(b1v);
      if (!tag_ok(gc, tag)) gc = wait_granule(ws, oc, tag);
      *(LAS unsigned*)(S + COLT + w * 512 + 256 + lane * 4) = b1v << 16;
      *(LAS unsigned*)(S + COLT + w * 512 + lane * 4) = (unsigned)gc;
      if (!tag_ok(g0, tag)) g0 = wait_granule(ws, o0, tag);
      if (!tag_ok(g1, tag)) g1 = wait_granule(ws, o0 + 32, tag);
      check_flags(ws, fwk, fw_seen, tag);
      PIN();
      rd(2, 0, 2); mm(0);
      rd(3, 0, 3); mm(1);
      rd(0, SLOT, 0); mm(2);
      rd(1, SLOT, 1); mm(3);
      rd(2, SLOT, 2); mm(0);
      rd(3, SLOT, 3); mm(1);
      mm(2);
      mm(3);
      STAMP(3);
      const unsigned hv = WS_HT + (((rp * 8 + 4 * tg + blk) * 54 + (c0 >> 5) + 2 * wf) * 64 + (lane & 15) + 16 * h) * 16 + ((cb == 13 && wf == 0) ? OOB : 0u);
#pragma unroll
      for (int c = 0; c < 2; ++c) {
        f4 cs[4], bb[4];
#pragma unroll
        for (int a = 0; a < 4; ++a) { LAS char* ct_ = S + COLT + w * 512 + (32 * c + 8 * a + 4 * h) * 4; cs[a] = *(LAS f4*)ct_; bb[a] = *(LAS f4*)(ct_ + 256); }
#pragma unroll
        for (int tt = 0; tt < 2; ++tt) {
          const float rs = __builtin_amdgcn_rcpf(bfhi((unsigned)(tt ? g1 : g0))), mr = -bflo((unsigned)(tt ? g1 : g0)) * rs;
          unsigned o[8];
#pragma unroll
          for (int a = 0; a < 4; ++a) {
#pragma unroll
            for (int e = 0; e < 2; ++e) {
              const f2 v = fma2(f2{acc[c][tt][4 * a + 2 * e], acc[c][tt][4 * a + 2 * e + 1]}, f2{rs, rs}, fma2(f2{mr, mr}, e ? cs[a].zw : cs[a].xy, e ? bb[a].zw : bb[a].xy));
              const f2 z = v * fma2(v * v, f2{-0.10294324f, -0.10294324f}, f2{-2.30220819f, -2.30220819f});
              const f2 dn = f2{__builtin_amdgcn_exp2f(z.x), __builtin_amdgcn_exp2f(z.y)} + f2{1.f, 1.f};
              o[2 * a + e] = pk2(v * f2{__builtin_amdgcn_rcpf(dn.x), __builtin_amdgcn_rcpf(dn.y)});
            }
          }
          load_w2_(w2s, NA * (2 * c + tt), NA * (2 * c + tt + 1));
          st16(rws, v4u{o[0], o[1], o[2], o[3]}, hv + (tt * 108 + c) * 1024);
          st16(rws, v4u{o[4], o[5], o[6], o[7]}, hv + (tt * 108 + c) * 1024 + 512);
          PIN();
        }
      }
      STAMP(4);
      lds_put(CNT + 16 * 6 + 4 * wq, 1);
      load_w2_(w2s, 4 * NA, 4 * NA + 8);
      WAITV(8);
      if (lds_arrive(S, 0, wq) && lane == 0) put_flag(ws, FL_H + gi, tag);
      STAMP(5);
      load_w2_(w2s, 4 * NA + 8, 35);
      auto publish = [&](int j) {
        lds_wait_ge(S, CNT + 16 * 2 + 4 * p, j + 1);
        const unsigned pa = (j & 1) * SLOT + (tid >> 5) * 1024 + (((tid >> 1) & 15) ^ (4 * (tid & 1))) * 16 + (tid & 1) * 512;
        f4 lo[4], hi[4], l, m;
#pragma unroll
        for (int v = 0; v < 4; ++v) { lo[v] = *(LAS f4*)(S + pa + v * 8192); hi[v] = *(LAS f4*)(S + (pa ^ 32u) + 256 + v * 8192); }
#pragma unroll
        for (int e = 0; e < 4; ++e) { l[e] = sadd(sadd(lo[0][e], lo[1][e]), sadd(lo[2][e], lo[3][e])); m[e] = sadd(sadd(hi[0][e], hi[1][e]), sadd(hi[2][e], hi[3][e])); }
        GOT(l); GOT(m);
        lds_put(CNT + 16 * 9 + 4 * wq, j + 1);
        st16(rws, v4u{pk2(l.xy), pk2(l.zw), pk2(m.xy), pk2(m.zw)}, tid * 16 + WS_YT + (j < 2 ? ch00 + j : ch3) * 4096);
      };
      auto first_h = [&]() {
        const v4u sw = mk_srd(ws, WS_END);
#pragma unroll
        for (int i = 0; i < 7; ++i) asm volatile("buffer_load_dwordx4 %0, %1, %2, %3 offen sc1" : "+v"(junk) : "v"(w2v), "s"(sw), "s"(hb0 + 4096 + (i < 6 || w < 2 ? i * 8192u : 0u)) : "memory");
        load_h(hb0);
        PIN();
      };
      publish(0);
      u64 f1 = ld_word(ws, WS_FLAG + 128u * fhk);
      if (nch == 3) publish(1);
      if (!__all(f1 == tag)) f1 = ld_word(ws, WS_FLAG + 128u * fhk);
      const bool early = __all(f1 == tag);
      settle_w2(35);
      __builtin_amdgcn_fence(__ATOMIC_ACQUIRE, "wavefront");
      if (early) first_h();
      publish(nch - 1);
      if (!early) {
        wait_flags(ws, fhk, tag);
        load_h(hb0);
      }
    } else {
      lds_arrive(S, 6, wq);
      lds_wait(S, 6);
      tag = get_tag(S);
#pragma unroll
      for (int j = 0; j < 6; ++j) st16(rws, *(LAS v4u*)(S + KARR + j * KCH + 512 + tid * 16), tid * 16 + WS_K3 + (ch00 + j) * 4096);
      WAITV(0);
      if (lds_arrive(S, 0, wq) && lane == 0) put_flag(ws, FL_K + sp, tag);
      if (WARMA) warm_w2(2 * (bi >> 3), tag);
      wait_flags(ws, fwk, tag);
      load_w2_(w2s, 0, 35);
      settle_w2(35);
      wait_flags(ws, fhk, tag);
      load_h(hb0);
    }
    STAMP(7);
  }
  {
    STAMP(8);
    IDS();
    PIN();
    setup_c(rt, ct);
    const v4u sws = mk_srd(ws, WS_END);
    f4 acc[2][5];
#define GROUP(WAIT, A, j, OFF)                                                                                             \
    asm volatile("s_waitcnt vmcnt(" #WAIT ")\n\tv_mfma_f32_16x16x32_bf16 %0, %6, %5, %0\n\tv_mfma_f32_16x16x32_bf16 %1, %7, %5, %1\n\t"  \
                 "v_mfma_f32_16x16x32_bf16 %2, %8, %5, %2\n\tv_mfma_f32_16x16x32_bf16 %3, %9, %5, %3\n\t"                      \
                 "v_mfma_f32_16x16x32_bf16 %4, %10, %5, %4\n\tbuffer_load_dwordx4 %5, %11, %12, %13 offen sc1"                \
                 : "+v"(A[0]), "+v"(A[1]), "+v"(A[2]), "+v"(A[3]), "+v"(A[4]), "+v"(hf[j])                                  \
                 : "v"(wf2[5 * j]), "v"(wf2[5 * j + 1]), "v"(wf2[5 * j + 2]), "v"(wf2[5 * j + 3]), "v"(wf2[5 * j + 4]), "v"(w2v), "s"(sws), "s"(OFF) : "memory")
#define GROUP0(WAIT, A, OFF)                                                                                               \
    asm volatile("s_waitcnt vmcnt(" #WAIT ")\n\tv_mfma_f32_16x16x32_bf16 %0, %6, %5, 0\n\tv_mfma_f32_16x16x32_bf16 %1, %7, %5, 0\n\t"    \
                 "v_mfma_f32_16x16x32_bf16 %2, %8, %5, 0\n\tv_mfma_f32_16x16x32_bf16 %3, %9, %5, 0\n\t"                        \
                 "v_mfma_f32_16x16x32_bf16 %4, %10, %5, 0\n\tbuffer_load_dwordx4 %5, %11, %12, %13 offen sc1"                 \
                 : "=&v"(A[0]), "=&v"(A[1]), "=&v"(A[2]), "=&v"(A[3]), "=&v"(A[4]), "+v"(hf[0])                             \
                 : "v"(wf2[0]), "v"(wf2[1]), "v"(wf2[2]), "v"(wf2[3]), "v"(wf2[4]), "v"(w2v), "s"(sws), "s"(OFF) : "memory")
#define LAST(WAIT, A, j, C)                                                                                                \
    asm volatile("s_waitcnt vmcnt(" #WAIT ")\n\tv_mfma_f32_16x16x32_bf16 %0, %6, %5, " C(0) "\n\tv_mfma_f32_16x16x32_bf16 %1, %7, %5, " C(1) "\n\t" \
                 "v_mfma_f32_16x16x32_bf16 %2, %8, %5, " C(2) "\n\tv_mfma_f32_16x16x32_bf16 %3, %9, %5, " C(3) "\n\t"          \
                 "v_mfma_f32_16x16x32_bf16 %4, %10, %5, " C(4)                                                              \
                 : "+v"(A[0]), "+v"(A[1]), "+v"(A[2]), "+v"(A[3]), "+v"(A[4])                                               \
                 : "v"(hf[j]), "v"(wf2[5 * j]), "v"(wf2[5 * j + 1]), "v"(wf2[5 * j + 2]), "v"(wf2[5 * j + 3]), "v"(wf2[5 * j + 4]) : "memory")
#define C_ACC(i) "%" #i
#define C_ZERO(i) "0"
#define PUT(buf, A, n) *(LAS f4*)(Pw + (buf) * GBUF + (n) * GT) = A[n]
    STAMP(9);
    GROUP0(6, acc[0], hb1); GROUP(6, acc[0], 1, hb1 + 8192); GROUP(6, acc[0], 2, hb1 + 16384); GROUP(6, acc[0], 3, hb1 + 24576);
    GROUP(6, acc[0], 4, hb1 + 32768); GROUP(6, acc[0], 5, hb1 + 40960); GROUP(6, acc[0], 6, hb1 + k6);
    GOT(junk);
    if (w < 4 && !spare) {
      WAITV(7);
      if (lds_arrive(S, 10, wq) && lane == 0) put_flag(ws, FL_Y + gi, tag);
    }
    STAMP(11);
    if (!spare) lds_wait_ge(S, CNT + 16 * 9 + 4 * p, nch);
    else bar();
    GROUP0(6, acc[1], hb2); PUT(0, acc[0], 0); GROUP(6, acc[1], 1, hb2 + 8192); PUT(0, acc[0], 1); GROUP(6, acc[1], 2, hb2 + 16384); PUT(0, acc[0], 2);
    GROUP(6, acc[1], 3, hb2 + 24576); PUT(0, acc[0], 3); GROUP(6, acc[1], 4, hb2 + 32768); PUT(0, acc[0], 4); GROUP(6, acc[1], 5, hb2 + 40960); GROUP(6, acc[1], 6, hb2 + k6);
    STAMP(14);
    GROUP0(6, acc[0], hb3); PUT(1, acc[1], 0); GROUP(6, acc[0], 1, hb3 + 8192); PUT(1, acc[1], 1); GROUP(6, acc[0], 2, hb3 + 16384); PUT(1, acc[1], 2);
    GROUP(6, acc[0], 3, hb3 + 24576); PUT(1, acc[1], 3); GROUP(6, acc[0], 4, hb3 + 32768); PUT(1, acc[1], 4); GROUP(6, acc[0], 5, hb3 + 40960); GROUP(6, acc[0], 6, hb3 + k6);
    fh = ld_hidden(ws, fy_off);
    v2u xr[3], bb[3];
    {
      const v4u sx = mk_srd(x, L * D * 2), sb = mk_srd(b2, D * 2);
#pragma unroll
      for (int i = 0; i < 3; ++i)
        asm volatile("buffer_load_dwordx2 %0, %2, %3, %4 offen\n\tbuffer_load_dwordx2 %1, %5, %6, %7 offen" : "=&v"(xr[i]), "=&v"(bb[i]) : "v"(ovb), "s"(sx), "s"(tos[i]), "v"(p * 8), "s"(sb), "s"(tbs[i]) : "memory");
    }
    bar();
    auto add4 = [](f4 a, f4 c) { return f4{sadd(a.x, c.x), sadd(a.y, c.y), sadd(a.z, c.z), sadd(a.w, c.w)}; };
    f4 p0[4], s0a;
#pragma unroll
    for (int v = 0; v < 4; ++v) p0[v] = *(LAS f4*)(S + prb + tps[0] + v * GW);
    LAST(13, acc[1], 0, C_ZERO); PUT(2, acc[0], 0); LAST(12, acc[1], 1, C_ACC); PUT(2, acc[0], 1);
    s0a = add4(add4(p0[0], p0[1]), add4(p0[2], p0[3])); GOT(s0a);
#pragma unroll
    for (int v = 0; v < 4; ++v) p0[v] = *(LAS f4*)(S + prb + tps[0] + (4 + v) * GW);
    LAST(11, acc[1], 2, C_ACC); PUT(2, acc[0], 2); LAST(10, acc[1], 3, C_ACC); PUT(2, acc[0], 3);
    s0a = add4(s0a, add4(add4(p0[0], p0[1]), add4(p0[2], p0[3]))); GOT(s0a);
    lds_put(CNT + 16 * 11 + 4 * w, 1);
    LAST(0, acc[1], 4, C_ACC); PUT(2, acc[0], 4);
    GOT(fh);
#pragma unroll
    for (int i = 0; i < 3; ++i) { GOT(xr[i]); GOT(bb[i]); }
    v4u yv[3];
    const bool y_early = __all(fh == tag);
    __builtin_amdgcn_fence(__ATOMIC_ACQUIRE, "wavefront");
    if (y_early) {
#pragma unroll
      for (int i = 0; i < 3; ++i) yv[i] = ld16(rws, ylb, tys[i]);
    }
    PIN();
    LAST(63, acc[1], 5, C_ACC); LAST(63, acc[1], 6, C_ACC);
    if (w < 4) STAMP(12);
    lds_wait_ge(S, CNT + 16 * 11 + 4 * (lane & 7), 1);
    asm volatile("s_nop 7" : "+v"(acc[1][0]), "+v"(acc[1][1]), "+v"(acc[1][2]), "+v"(acc[1][3]), "+v"(acc[1][4]));
#pragma unroll
    for (int n = 0; n < 5; ++n) PUT(0, acc[1], n);
    if (!y_early) {
#pragma unroll 1
      for (unsigned s = 0; s < SPIN_CAP; ++s) { if (__all(ld_word(ws, fy_off) == tag)) break; __builtin_amdgcn_s_sleep(1); }
      __builtin_amdgcn_fence(__ATOMIC_ACQUIRE, "wavefront");
#pragma unroll
      for (int i = 0; i < 3; ++i) yv[i] = ld16(rws, ylb, tys[i]);
    }
    bar();
    if (w < 4) STAMP(13);
    const __amdgpu_buffer_rsrc_t ro = __builtin_amdgcn_make_buffer_rsrc(out, 0, L * D * 2, 0x00020000);
    f4 pr[2][8];
#pragma unroll
    for (int v = 0; v < 16; ++v) pr[v >> 3][v & 7] = *(LAS f4*)(S + prb + tps[1 + (v >> 3)] + (v & 7) * GW);
    PIN();
    v2u yb[3];
#pragma unroll
    for (int i = 0; i < 3; ++i) {
      const f4 sa = i == 0 ? s0a : ((pr[i - 1][0] + pr[i - 1][1]) + (pr[i - 1][2] + pr[i - 1][3])) + ((pr[i - 1][4] + pr[i - 1][5]) + (pr[i - 1][6] + pr[i - 1][7]));
      yb[i] = v2u{pk2(unpk(pk2(sa.xy)) + unpk(bb[i].x)), pk2(unpk(pk2(sa.zw)) + unpk(bb[i].y))};
    }
#pragma unroll
    for (int i = 0; i < 3; ++i) {
      const unsigned e = (lane & 15) + tsel[i] - 8 * g, one = e < 8u ? 0x3f80u << (16 * (e & 1)) : 0u;
      const v4u sel = {(e & 6) == 0 ? one : 0u, (e & 6) == 2 ? one : 0u, (e & 6) == 4 ? one : 0u, (e & 6) == 6 ? one : 0u};
      const f4 yt = __builtin_amdgcn_mfma_f32_16x16x32_bf16(asbf(yv[i]), asbf(sel), f4{0.f, 0.f, 0.f, 0.f}, 0, 0, 0);
      const int from = ((lane >> 2) + 16 * p) * 4;
      const unsigned y0 = __builtin_amdgcn_ds_bpermute(from, pk2(yt.xy)), y1 = __builtin_amdgcn_ds_bpermute(from, pk2(yt.zw));
      __builtin_amdgcn_raw_buffer_store_b64(v2u{pk2(unpk(pk2(unpk(xr[i].x) + unpk(y0))) + unpk(yb[i].x)), pk2(unpk(pk2(unpk(xr[i].y) + unpk(y1))) + unpk(yb[i].y))}, ro,
                                            ovb + (i == 0 || (i == 1 ? w < 7 : w < 5) ? 0u : OOB), tos[i], 0);
    }
  }
  STAMP(10);
  STAMP_OUT();
  if (b == 0 && w == 7) {
#pragma unroll 1
    for (unsigned s = 0, j = 0; s < SPIN_CAP && j < 4; ++s)
      if (__all((unsigned)(ld_word(ws, WS_ST + (64 * j + lane) * 8) >> 32) == (unsigned)tag)) ++j;
    if (lane == 0) __hip_atomic_store((GAS u64*)ws, tag, __ATOMIC_RELAXED, __HIP_MEMORY_SCOPE_AGENT);
  }
  asm volatile("s4_code_end:");
}
}

extern "C" void kernel_launch(void* const* d_in, const int* in_sizes, int n_in, void* d_out, int out_size, void* d_ws, size_t ws_size,
                              hipStream_t stream) {
  (void)in_sizes; (void)n_in; (void)out_size; (void)ws_size;
  (void)hipFuncSetAttribute((const void*)s4mlp_kernel, hipFuncAttributeMaxDynamicSharedMemorySize, LDS_BYTES);
  s4mlp_kernel<<<256, 512, LDS_BYTES, stream>>>((const u16*)d_in[0], (const u16*)d_in[1], (const u16*)d_in[2], (const u16*)d_in[3], (const u16*)d_in[4],
                                                (const u16*)d_in[5], (const u16*)d_in[6], (const u16*)d_in[11], (const u16*)d_in[7], (const u16*)d_in[8],
                                                (const u16*)d_in[10], (const u16*)d_in[12], (const u16*)d_in[13], (u16*)d_out, (char*)d_ws);
}
```
